```python
import torch

def _softmax(x, axis=-1):
    return torch.softmax(x, dim=axis)
def _gelu(x):
    return torch.nn.functional.gelu(x, approximate="tanh")
def _silu(x):
    return torch.nn.functional.silu(x)
def _sigmoid(x):
    return torch.sigmoid(x)
def _layernorm(x, axis=-1, eps=1e-5):
    m = x.mean(dim=axis, keepdim=True)
    v = x.var(dim=axis, keepdim=True, unbiased=False)
    return (x - m) / torch.sqrt(v + eps)
def _scatter_add(tgt, idx, src, axis=0):
    return tgt.clone().index_add_(axis, idx, src)
def _segment_sum(src, seg, n):
    out = torch.zeros((n,) + src.shape[1:], dtype=src.dtype)
    return out.index_add_(0, seg, src)
def _pad(x, pw):
    flat = tuple(v for pair in reversed(pw) for v in pair)
    return torch.nn.functional.pad(x, flat)

kernel_name = 'recurrent_scan__linear_kernel_hedgehog__glu_gated_bilinear__prompt'

def setup_inputs():

    _w = (lambda a: (lambda t: t.to(torch.bfloat16) if t.is_floating_point() else t)(torch.as_tensor(a)))
    def _A_setup():
        B, S, D, H = 1, 2048, 512, 32
        Dh = D // H
        return {
            "x": _w((lambda *s: torch.randn(*s, dtype=torch.bfloat16) * 0.05)(B, S, D)),
            "wqkv": _w((lambda *s: torch.randn(*s, dtype=torch.bfloat16) * 0.8)(D, 3 * D)),
            "w_phi": _w((lambda *s: torch.randn(*s, dtype=torch.bfloat16) * 0.8)(H, Dh, Dh) * (Dh ** -0.5)),
            "wo": _w((lambda *s: torch.randn(*s, dtype=torch.bfloat16) * 0.8)(D, D)),
        }
    _A_in = _A_setup()

    def _B_setup():
        B, S, D, F = 1, 2048, 512, 1792
        return {
            "x": _w((lambda *s: torch.randn(*s, dtype=torch.bfloat16) * 0.05)(B, S, D)),
            "w_gate": _w((lambda *s: torch.randn(*s, dtype=torch.bfloat16) * 0.2)(D, F) * (D ** -0.5)),
            "w_up": _w((lambda *s: torch.randn(*s, dtype=torch.bfloat16) * 0.2)(D, F) * (D ** -0.5)),
            "w_down": _w((lambda *s: torch.randn(*s, dtype=torch.bfloat16) * 0.2)(F, D) * (F ** -0.5)),
        }
    _B_in = _B_setup()

    return {
        "A_x": _A_in["x"],
        "A_wqkv": _A_in["wqkv"],
        "A_w_phi": _A_in["w_phi"],
        "A_wo": _A_in["wo"],
        "B_x": _B_in["x"],
        "B_w_gate": _B_in["w_gate"],
        "B_w_up": _B_in["w_up"],
        "B_w_down": _B_in["w_down"],
    }

def reference(A_x, A_wqkv, A_w_phi, A_wo, B_x, B_w_gate, B_w_up, B_w_down):
    def _A_ref(x, wqkv, w_phi, wo):
        B, S, D = x.shape
        H = 32; Dh = D // H
        qkv = (lambda x, *d: x.permute(*d))((x @ wqkv).reshape(B, S, 3, H, Dh), 2, 0, 3, 1, 4)
        q, k, v = qkv[0], qkv[1], qkv[2]
        pq = torch.einsum("bhsd,hdf->bhsf", q, w_phi)
        pk = torch.einsum("bhsd,hdf->bhsf", k, w_phi)
        phi_q = torch.concatenate([_softmax(pq, axis=-1), _softmax(-pq, axis=-1)], axis=-1)
        phi_k = torch.concatenate([_softmax(pk, axis=-1), _softmax(-pk, axis=-1)], axis=-1)
        kv = torch.einsum("bhsf,bhsd->bhfd", phi_k, v)
        z  = torch.einsum("bhsf->bhf", phi_k)
        num = torch.einsum("bhqf,bhfd->bhqd", phi_q, kv)
        den = torch.einsum("bhqf,bhf->bhq", phi_q, z)[..., None] + 1e-6
        o = num / den
        return (lambda x, *d: x.permute(*d))(o, 0, 2, 1, 3).reshape(B, S, D) @ wo

    def _B_ref(x, w_gate, w_up, w_down):
        g = torch.einsum("bsd,df->bsf", x, w_gate)
        u = torch.einsum("bsd,df->bsf", x, w_up)
        h = g * u
        return torch.einsum("bsf,fd->bsd", h, w_down)

    x = A_x
    xn = _layernorm(x, axis=-1)
    s = _A_ref(xn, A_wqkv, A_w_phi, A_wo)
    g = _silu(_B_ref(xn, B_w_gate, B_w_up, B_w_down))
    return x + s * g
```

```cpp
#include <hip/hip_runtime.h>
#include <hip/hip_bf16.h>
#include <stdint.h>

typedef unsigned long long u64;
typedef __attribute__((ext_vector_type(8))) __bf16 b16x8;
typedef __attribute__((ext_vector_type(2))) __bf16 b16x2;
typedef __attribute__((ext_vector_type(4))) short s16x4;
typedef __attribute__((ext_vector_type(2))) float f32x2;
typedef __attribute__((ext_vector_type(4))) float f32x4;
typedef __attribute__((ext_vector_type(16))) float f32x16;
typedef __attribute__((ext_vector_type(2))) unsigned u32x2;
typedef __attribute__((ext_vector_type(4))) unsigned u32x4;
typedef __attribute__((ext_vector_type(4))) int i32x4;
#define LAS __attribute__((address_space(3)))
#define GAS __attribute__((address_space(1)))
#define DI __device__ __forceinline__

constexpr int LD_X = 1024, LD_QKV = 3072, LD_GU = 3584, LD_W2 = 1024;
constexpr int NKS = 144;
constexpr size_t WS_AFRAG = 0;
constexpr size_t WS_KVP = 64ull * NKS * 1024;
constexpr int KVP_BYTES = 4608;
constexpr size_t WS_FLAG = WS_KVP + 8 * 16 * KVP_BYTES;
constexpr size_t WS_EPOCH = WS_FLAG + 8 * 128;

constexpr int L_X = 0;
constexpr int L_PK = 0;
constexpr int L_V = 8 * 4352;
constexpr int L_PHI = 53248;
constexpr int L_CS = 55296;
constexpr int L_ST = 58368;
constexpr int L_KVT = 60416;
constexpr int L_W = 65536;
constexpr int LDS1 = 163840;
constexpr int LDS2 = 131072;

struct Args {
  const char* x; const char* wqkv; const char* wphi; const char* wo;
  const char* wg; const char* wu; const char* wd;
  char* ws; char* out;
};

DI i32x4 make_srd(const char* p, unsigned bytes) {
  const u64 a = (u64)p;
  i32x4 r;
  r[0] = __builtin_amdgcn_readfirstlane((int)(unsigned)a);
  r[1] = __builtin_amdgcn_readfirstlane((int)(unsigned)(a >> 32) & 0xffff);
  r[2] = (int)bytes;
  r[3] = 0x00020000;
  return r;
}
DI void dma16(unsigned lds, unsigned voff, const i32x4& srd, unsigned soff) {
  asm volatile("s_mov_b32 m0, %0\n\ts_nop 0\n\tbuffer_load_dwordx4 %1, %2, %3 offen lds"
               :: "s"(lds), "v"(voff), "s"(srd), "s"(soff) : "memory");
}
DI void dma4(unsigned lds, unsigned voff, const i32x4& srd, unsigned soff) {
  asm volatile("s_mov_b32 m0, %0\n\ts_nop 0\n\tbuffer_load_dword %1, %2, %3 offen lds"
               :: "s"(lds), "v"(voff), "s"(srd), "s"(soff) : "memory");
}
DI void dma16g(unsigned lds, const char* gptr) {
  asm volatile("s_mov_b32 m0, %0\n\ts_nop 0\n\tglobal_load_lds_dwordx4 %1, off" :: "s"(lds), "v"(gptr) : "memory");
}
template <int N> DI void wait_vm() { asm volatile("s_waitcnt vmcnt(%0)" :: "n"(N) : "memory"); }
DI void wg_barrier() {
  asm volatile("s_waitcnt lgkmcnt(0)" ::: "memory");
  __builtin_amdgcn_s_barrier();
  asm volatile("" ::: "memory");
}
DI u32x4 lds_r128(unsigned a) { return *(const LAS u32x4*)a; }
DI void lds_w128(unsigned a, u32x4 v) { *(LAS u32x4*)a = v; }
DI u32x4 lds_tr2(unsigned a0, unsigned a1) {
  const s16x4 lo = __builtin_amdgcn_ds_read_tr16_b64_v4i16((LAS s16x4*)a0);
  const s16x4 hi = __builtin_amdgcn_ds_read_tr16_b64_v4i16((LAS s16x4*)a1);
  const u32x2 l = __builtin_bit_cast(u32x2, lo), h = __builtin_bit_cast(u32x2, hi);
  return u32x4{l[0], l[1], h[0], h[1]};
}
DI f32x16 mfma32(u32x4 a, u32x4 b, f32x16 c) {
  return __builtin_amdgcn_mfma_f32_32x32x16_bf16(__builtin_bit_cast(b16x8, a), __builtin_bit_cast(b16x8, b), c, 0, 0, 0);
}
DI f32x4 mfma16(u32x4 a, u32x4 b, f32x4 c) {
  return __builtin_amdgcn_mfma_f32_16x16x32_bf16(__builtin_bit_cast(b16x8, a), __builtin_bit_cast(b16x8, b), c, 0, 0, 0);
}
DI float dot2(unsigned a, unsigned b, float c) {
  return __builtin_amdgcn_fdot2_f32_bf16(__builtin_bit_cast(b16x2, a), __builtin_bit_cast(b16x2, b), c, false);
}
DI unsigned pack2(float lo, float hi) {
  const b16x2 v = __builtin_convertvector(f32x2{lo, hi}, b16x2);
  return __builtin_bit_cast(unsigned, v);
}
DI float bf_lo(unsigned v) { return __builtin_bit_cast(float, v << 16); }
DI float bf_hi(unsigned v) { return __builtin_bit_cast(float, v & 0xffff0000u); }
DI float asf(unsigned v) { return __builtin_bit_cast(float, v); }
DI unsigned asu(float v) { return __builtin_bit_cast(unsigned, v); }
DI float half_sum(float v) {
  const auto r = __builtin_amdgcn_permlane32_swap(asu(v), asu(v), false, false);
  return asf(r[0]) + asf(r[1]);
}
DI float half_max(float v) {
  const auto r = __builtin_amdgcn_permlane32_swap(asu(v), asu(v), false, false);
  return fmaxf(asf(r[0]), asf(r[1]));
}
template <int CTRL> DI float dpp(float v) {
  return asf((unsigned)__builtin_amdgcn_update_dpp(0, (int)asu(v), CTRL, 0xf, 0xf, true));
}
DI u32x4 pack8(const f32x16& a, int o, float s) {
  return u32x4{pack2(a[o] * s, a[o + 1] * s), pack2(a[o + 2] * s, a[o + 3] * s),
               pack2(a[o + 4] * s, a[o + 5] * s), pack2(a[o + 6] * s, a[o + 7] * s)};
}
constexpr unsigned ONES2 = 0x3f803f80u;

DI void dual_softmax(const f32x16& p, int o, u32x4& pos, u32x4& neg) {
  const float L2E = 1.44269504088896f;
  float mx = p[o], mn = p[o];
#pragma unroll
  for (int i = 1; i < 8; ++i) { mx = fmaxf(mx, p[o + i]); mn = fminf(mn, p[o + i]); }
  mx = half_max(mx);
  mn = -half_max(-mn);
  const float mxl = mx * L2E, mnl = mn * L2E;
  float ep[8], en[8], sp = 0.f, sn = 0.f;
#pragma unroll
  for (int i = 0; i < 8; ++i) {
    ep[i] = __builtin_amdgcn_exp2f(fmaf(p[o + i], L2E, -mxl));
    en[i] = __builtin_amdgcn_exp2f(fmaf(p[o + i], -L2E, mnl));
    sp += ep[i]; sn += en[i];
  }
  const float rp = __builtin_amdgcn_rcpf(half_sum(sp)), rn = __builtin_amdgcn_rcpf(half_sum(sn));
#pragma unroll
  for (int i = 0; i < 4; ++i) {
    pos[i] = pack2(ep[2 * i] * rp, ep[2 * i + 1] * rp);
    neg[i] = pack2(en[2 * i] * rn, en[2 * i + 1] * rn);
  }
}

template <bool A> constexpr int n_units(int g) { return g < 8 ? 3 : g < 16 ? (A ? 1 : 3) : 0; }
constexpr int n_x(int j) { return j < 8 ? 2 : 0; }
template <bool A> constexpr int vm_at(int g) {
  return g < 8 ? n_units<A>(g + 1) + n_x(g + 1) + n_units<A>(g + 2) : n_units<A>(g + 1) + n_units<A>(g + 2);
}

struct K1 {
  i32x4 sx, s0, s1;
  unsigned vx0, vx1, vw;
  const char* vmix;
  unsigned xrow;
  unsigned col;
  unsigned w1k;
  unsigned xa[4];
  unsigned wa[2][4];
};
#define SB() __builtin_amdgcn_sched_barrier(0)

DI void issue_x(const K1& c, int j, int n) {
  if (j < 8) dma16(L_X + (j % 3) * 16384 + 2 * c.w1k + n * 1024, n ? c.vx1 : c.vx0, c.sx, c.xrow + 128 * j);
}
template <bool A> DI void issue_w(K1& c, int g, int u) {
  if (u >= n_units<A>(g)) return;
  const unsigned l = L_W + (g & 3) * 24576 + u * 8192 + c.w1k;
  const unsigned k0 = (g & 7) * 64;
  if (A) {
    if (g < 8) {
      dma16(l, c.vw, c.s0, c.col + k0 * LD_QKV + (u == 0 ? 1024 : u == 1 ? 2048 : 0));
    } else {
      dma16g(l, c.vmix);
      c.vmix += 64 * LD_GU;
    }
  } else {
    const int n = (g < 8 ? 0 : 3) + u;
    dma16(l, c.vw, (n & 1) ? c.s1 : c.s0, c.col + k0 * LD_GU + (n >> 1) * 128);
  }
}
template <bool A> DI void refill(K1& c, int g, int n) {
  if (n < 2) issue_x(c, g + 2, n);
  else issue_w<A>(c, g + 3, n - 2);
}
template <bool A> DI void open_stage(int g) {
  switch (vm_at<A>(g)) {
    case 0: wait_vm<0>(); break;
    case 1: wait_vm<1>(); break;
    case 2: wait_vm<2>(); break;
    case 3: wait_vm<3>(); break;
    case 6: wait_vm<6>(); break;
    default: wait_vm<8>(); break;
  }
  wg_barrier();
}
DI u32x4 read_w(const K1& c, int g, int i, int u) {
  const unsigned a = c.wa[(g >> 1) & 1][i] + (g & 1) * 24576 + u * 8192;
  return lds_tr2(a, a + 512);
}

template <bool A, int NU, int G0, bool OPEN_NEXT>
DI void gemm_pass(K1& c, u32x4 (&xf)[32], f32x16 (&acc)[NU], float (&cs)[NU], float& sum, float& sq) {
  u32x4 wf[NU];
  if (G0 == 0) xf[0] = lds_r128(c.xa[0]);
#pragma unroll
  for (int u = 0; u < NU; ++u) {
    wf[u] = read_w(c, G0, 0, u);
    cs[u] = 0.f;
#pragma unroll
    for (int e = 0; e < 16; ++e) acc[u][e] = 0.f;
  }
  SB();
#pragma unroll
  for (int idx = 0; idx < 32; ++idx) {
    const int g = G0 + idx / 4, i = idx & 3;
    const int gn = G0 + (idx + 1) / 4, in = (idx + 1) & 3;
    const bool more = idx + 1 < 32;
#pragma unroll
    for (int u = 0; u < NU; ++u) {
      acc[u] = mfma32(wf[u], xf[idx], acc[u]);
      if (i == 0) {
#pragma unroll
        for (int e = 0; e < 4; ++e) cs[u] = dot2(wf[u][e], ONES2, cs[u]);
        asm volatile("" : "+v"(cs[u]));
      }
      SB();
      if (u == 0 && i == 3 && (more || OPEN_NEXT)) { open_stage<A>(g + 1); SB(); }
      if (more) {
        if (u == 0 && G0 == 0) xf[idx + 1] = lds_r128(c.xa[in] + (gn % 3) * 16384);
        wf[u] = read_w(c, gn, in, u);
      }
      if (G0 == 0 && (i == 1 || i == 2) && u < 2) {
#pragma unroll
        for (int e = 2 * u; e < 2 * u + 2; ++e) { sum = dot2(xf[idx][e], ONES2, sum); sq = dot2(xf[idx][e], xf[idx][e], sq); }
        asm volatile("" : "+v"(sum), "+v"(sq));
      }
      if (NU == 3) {
        if (i == 3 && u > 0 && (more || OPEN_NEXT)) refill<A>(c, g + 1, u - 1);
        if (i < 3 && u == 2) refill<A>(c, g, i + 2);
      } else {
        if (i == 0) refill<A>(c, g, 2);
      }
      SB();
    }
  }
}

template <int NU> DI void put_colsum(const float (&cs)[NU], int wm, int wn, int r) {
#pragma unroll
  for (int u = 0; u < NU; ++u) {
    const float t = half_sum(cs[u]);
    const float hi = bf_lo(pack2(t, 0.f));
    *(LAS unsigned*)(L_CS + ((u * 2 + wn) * 32 + r) * 16 + wm * 4) = pack2(hi, t - hi);
  }
}
template <int NU> DI void apply_mean(f32x16 (&acc)[NU], u32x4 negm, int wn, int r) {
#pragma unroll
  for (int u = 0; u < NU; ++u) acc[u] = mfma32(lds_r128(L_CS + ((u * 2 + wn) * 32 + r) * 16), negm, acc[u]);
}
DI void store_frag(char* ws, int tb, int ks, int lane, u32x4 v) {
  *(GAS u32x4*)(ws + WS_AFRAG + ((size_t)(tb * NKS + ks) * 64 + lane) * 16) = v;
}
DI void store_h(char* ws, int tb, int ks, int lane, const f32x16& g, const f32x16& u, float r2) {
  f32x16 h;
#pragma unroll
  for (int e = 0; e < 16; ++e) h[e] = g[e] * u[e];
  store_frag(ws, tb, ks, lane, pack8(h, 0, r2));
  store_frag(ws, tb, ks + 1, lane, pack8(h, 8, r2));
}

template <bool A>
DI void k1_body(const Args& p, int rt, int ct) {
  const int tid = threadIdx.x, lane = tid & 63, w = __builtin_amdgcn_readfirstlane(tid >> 6);
  const int wm = w & 3, wn = w >> 2, r = lane & 31, hh = lane >> 5;
  const int blk = (lane >> 4) & 1, q4 = (lane & 15) >> 2, p4 = lane & 3;
  const int tb = rt * 4 + wm;

  u64 epoch = 0;
  GAS u64* const flags = (GAS u64*)(p.ws + WS_FLAG) + ct * 16;
  GAS u64* const epoch_word = (GAS u64*)(p.ws + WS_EPOCH) + ct * 16;
  if (A) epoch = __hip_atomic_load(epoch_word, __ATOMIC_RELAXED, __HIP_MEMORY_SCOPE_AGENT);

  K1 c;
  c.sx = make_srd(p.x, 2048 * LD_X);
  c.w1k = w * 1024;
  c.xrow = rt * 128 * LD_X;
  {
    const int row = 16 * w + (lane >> 3), ch = (lane & 7) ^ ((row >> 1) & 7);
    c.vx0 = row * LD_X + ch * 16;
    c.vx1 = (row + 8) * LD_X + (ch ^ 4) * 16;
    const int wrow = 8 * w + (lane >> 3), wch = (lane & 7) ^ (((lane >> 4) & 1) << 2);
    if (A) {
      c.s0 = make_srd(p.wqkv, 512 * LD_QKV); c.s1 = c.s0;
      c.vw = wrow * LD_QKV + wch * 16;
      c.col = ct * 128;
      c.vmix = (((wch >> 1) & 1) ? p.wu : p.wg) + wrow * LD_GU + (1536 + 32 * ct + 16 * (wch >> 2) + 8 * (wch & 1)) * 2;
    } else {
      c.s0 = make_srd(p.wg, 512 * LD_GU); c.s1 = make_srd(p.wu, 512 * LD_GU);
      c.vw = wrow * LD_GU + wch * 16;
      c.col = ct * 384;
      c.vmix = nullptr;
    }
    const int rot = wm + 2 * wn;
#pragma unroll
    for (int i = 0; i < 4; ++i) {
      const int s = (i + rot) & 3;
      c.xa[i] = L_X + (32 * wm + r) * 128 + (((2 * s + hh) ^ ((r >> 1) & 7)) * 16);
      c.wa[0][i] = L_W + 2048 * s + (8 * hh + q4) * 128 + ((wn ^ (q4 >> 1)) * 64) + 32 * blk + 8 * p4;
      c.wa[1][i] = c.wa[0][i] + 49152;
      asm volatile("" : "+v"(c.wa[0][i]), "+v"(c.wa[1][i]));
    }
  }
  if (A) dma4(L_PHI + w * 256, tid * 4, make_srd(p.wphi, 32 * 512), ct * 2048);
#pragma unroll
  for (int g = 0; g < 3; ++g) {
#pragma unroll
    for (int u = 0; u < 3; ++u) issue_w<A>(c, g, u);
    if (g < 2) { issue_x(c, g, 0); issue_x(c, g, 1); }
  }
  open_stage<A>(0);
  refill<A>(c, 0, 0); refill<A>(c, 0, 1);

  u32x4 xf[32];
  f32x16 acc[3];
  float cs[3], sum = 0.f, sq = 0.f;
  gemm_pass<A, 3, 0, true>(c, xf, acc, cs, sum, sq);

  put_colsum<3>(cs, wm, wn, r);
  sum = half_sum(sum); sq = half_sum(sq);
  *(LAS f32x2*)(L_ST + ((wm * 2 + wn) * 32 + r) * 8) = f32x2{sum, sq};
  wg_barrier();
  float rstd;
  u32x4 negm;
  {
    const f32x2 o = *(const LAS f32x2*)(L_ST + ((wm * 2 + (wn ^ 1)) * 32 + r) * 8);
    const float mean = (sum + o[0]) * (1.f / 512.f);
    const float var = fmaxf((sq + o[1]) * (1.f / 512.f) - mean * mean, 0.f);
    rstd = __builtin_amdgcn_rsqf(var + 1e-5f);
    const float mhi = bf_lo(pack2(-mean, 0.f));
    const float mm = hh ? (-mean - mhi) : mhi;
    const unsigned m2 = pack2(mm, mm);
    negm = u32x4{m2, m2, m2, m2};
  }
  apply_mean<3>(acc, negm, wn, r);
  const float r2 = rstd * rstd;

  if constexpr (!A) {
    const int ks0 = 12 * ct + 2 * wn;
    store_h(p.ws, tb, ks0, lane, acc[0], acc[1], r2);
    const f32x16 g1 = acc[2];
    float dummy0 = 0.f, dummy1 = 0.f;
    gemm_pass<A, 3, 8, false>(c, xf, acc, cs, dummy0, dummy1);
    put_colsum<3>(cs, wm, wn, r);
    wg_barrier();
    apply_mean<3>(acc, negm, wn, r);
    store_h(p.ws, tb, ks0 + 4, lane, g1, acc[0], r2);
    store_h(p.ws, tb, ks0 + 8, lane, acc[1], acc[2], r2);
  } else {
    u32x4 ph0, ph1;
    {
      const unsigned a = L_PHI + (2 * wn + blk) * 512 + (4 * hh + q4) * 32 + 8 * p4;
      const u32x4 wp = lds_tr2(a, a + 256);
      const u32x4 z = {0u, 0u, 0u, 0u};
      ph0 = blk ? z : wp;
      ph1 = blk ? wp : z;
    }
    const f32x16 zero16 = {};
    const unsigned img = hh * 2176 + (32 * wm + r) * 16;
    {
      f32x16 pk = mfma32(ph0, pack8(acc[0], 0, rstd), zero16);
      pk = mfma32(ph1, pack8(acc[0], 8, rstd), pk);
#pragma unroll
      for (int s = 0; s < 2; ++s) {
        u32x4 pos, neg;
        dual_softmax(pk, 8 * s, pos, neg);
        lds_w128(L_PK + (2 * wn + s) * 4352 + img, pos);
        lds_w128(L_PK + (4 + 2 * wn + s) * 4352 + img, neg);
        lds_w128(L_V + (2 * wn + s) * 4352 + img, pack8(acc[1], 8 * s, rstd));
      }
    }
    wg_barrier();
    char* const kvp = p.ws + WS_KVP + (size_t)ct * 16 * KVP_BYTES;
    const __amdgpu_buffer_rsrc_t rkv = __builtin_amdgcn_make_buffer_rsrc(kvp, 0, 16 * KVP_BYTES, 0x00020000);
    {
      const unsigned t = (p4 >> 1) * 2176 + (4 * (lane >> 4) + q4) * 16 + (p4 & 1) * 8;
      const u32x4 ones = {ONES2, ONES2, ONES2, ONES2};
      f32x4 kv = {}, z = {};
#pragma unroll
      for (int ks = 0; ks < 4; ++ks) {
        const unsigned ap = L_PK + w * 4352 + t + ks * 512, av = L_V + (w & 3) * 4352 + t + ks * 512;
        const u32x4 fa = lds_tr2(ap, ap + 256), fb = lds_tr2(av, av + 256);
        kv = mfma16(fa, fb, kv);
        z = mfma16(fa, ones, z);
      }
      __builtin_amdgcn_raw_buffer_store_b64(u32x2{pack2(kv[0], kv[1]), pack2(kv[2], kv[3])}, rkv,
                                            rt * KVP_BYTES + tid * 8, 0, 16);
      if ((lane & 15) == 0)
        __builtin_amdgcn_raw_buffer_store_b128(__builtin_bit_cast(u32x4, z), rkv,
                                               rt * KVP_BYTES + 4096 + w * 64 + (lane >> 4) * 16, 0, 16);
    }
    u32x4 fq[2][2];
    {
      f32x16 pq = mfma32(ph0, pack8(acc[2], 0, rstd), zero16);
      pq = mfma32(ph1, pack8(acc[2], 8, rstd), pq);
      dual_softmax(pq, 0, fq[0][0], fq[0][1]);
      dual_softmax(pq, 8, fq[1][0], fq[1][1]);
    }
    epoch += 1;
    wait_vm<0>();
    wg_barrier();
    if (tid == 0) __hip_atomic_store(flags + rt, epoch, __ATOMIC_RELAXED, __HIP_MEMORY_SCOPE_AGENT);

    f32x16 am[1];
    float csm[1], dummy0 = 0.f, dummy1 = 0.f;
    gemm_pass<A, 1, 8, false>(c, xf, am, csm, dummy0, dummy1);
    put_colsum<1>(csm, wm, wn, r);
    if (w == 0) {
      for (unsigned spin = 0; spin < (1u << 22); ++spin) {
        const u64 f = __hip_atomic_load(flags + (lane & 15), __ATOMIC_RELAXED, __HIP_MEMORY_SCOPE_AGENT);
        if (__all(f == epoch)) break;
        __builtin_amdgcn_s_sleep(1);
      }
      __builtin_amdgcn_fence(__ATOMIC_ACQUIRE, "wavefront");
      if (rt == 0 && lane == 0) __hip_atomic_store(epoch_word, epoch, __ATOMIC_RELAXED, __HIP_MEMORY_SCOPE_AGENT);
    }
    wg_barrier();
    u32x4 part[8];
#pragma unroll
    for (int i = 0; i < 8; ++i)
      part[i] = __builtin_amdgcn_raw_buffer_load_b128(rkv, (2 * i + (lane & 1)) * KVP_BYTES + (tid >> 1) * 16, 0, 16);
    const u32x4 zpart = __builtin_amdgcn_raw_buffer_load_b128(rkv, (lane & 15) * KVP_BYTES + 4096 + w * 64 + (lane >> 4) * 16, 0, 16);

    apply_mean<1>(am, negm, wn, r);
    {
      f32x16 h;
#pragma unroll
      for (int e = 0; e < 8; ++e) h[e] = am[0][e] * am[0][8 + e];
      store_frag(p.ws, tb, 96 + 2 * ct + wn, lane, pack8(h, 0, r2));
    }
    {
      float v[8] = {};
#pragma unroll
      for (int i = 0; i < 8; ++i)
#pragma unroll
        for (int e = 0; e < 4; ++e) { v[2 * e] += bf_lo(part[i][e]); v[2 * e + 1] += bf_hi(part[i][e]); }
      float t[4];
#pragma unroll
      for (int e = 0; e < 4; ++e) {
        const float give = (lane & 1) ? v[e] : v[4 + e], keep = (lane & 1) ? v[4 + e] : v[e];
        t[e] = keep + dpp<0xB1>(give);
      }
      const int dl = lane & 15, d = 8 * ((dl & 7) >> 2) + 4 * (dl >> 3) + (dl & 3);
      *(LAS u32x2*)(L_KVT + w * 544 + d * 32 + (lane >> 4) * 8) = u32x2{pack2(t[0], t[1]), pack2(t[2], t[3])};
      f32x4 z = __builtin_bit_cast(f32x4, zpart);
#pragma unroll
      for (int e = 0; e < 4; ++e) {
        float s = z[e];
        s += dpp<0xB1>(s); s += dpp<0x4E>(s); s += dpp<0x141>(s); s += dpp<0x140>(s);
        z[e] = s;
      }
      if (dl == 0) *(LAS u32x2*)(L_KVT + w * 544 + 512 + (lane >> 4) * 8) = u32x2{pack2(z[0], z[1]), pack2(z[2], z[3])};
    }
    wg_barrier();
#pragma unroll
    for (int s = 0; s < 2; ++s) {
      const unsigned a = L_KVT + (2 * wn + s) * 544 + (r < 16 ? r : 16) * 32 + 16 * hh;
      f32x16 o = mfma32(lds_r128(a), fq[s][0], zero16);
      o = mfma32(lds_r128(a + 4 * 544), fq[s][1], o);
      store_frag(p.ws, tb, 112 + 4 * ct + 2 * wn + s, lane, pack8(o, 0, __builtin_amdgcn_rcpf(o[8] + 1e-6f)));
    }
  }
}

__global__ __launch_bounds__(512, 2) void hh_proj_kernel(Args p) {
  extern __shared__ __attribute__((aligned(16))) char smem[];
  const int xcd = blockIdx.x & 7, j = blockIdx.x >> 3;
  const int rt = 4 * (xcd & 3) + (j & 3), ct = 4 * (xcd >> 2) + (j >> 3);
  if ((j >> 2) & 1) k1_body<true>(p, rt, ct);
  else k1_body<false>(p, rt, ct);
}

__global__ __launch_bounds__(512, 2) void hh_out_kernel(Args p) {
  extern __shared__ __attribute__((aligned(16))) char smem[];
  const int tid = threadIdx.x, lane = tid & 63, w = __builtin_amdgcn_readfirstlane(tid >> 6);
  const int kh = w >> 2, v = w & 3, wm = w & 1, wn = (w >> 1) & 1, r = lane & 31, hh = lane >> 5;
  const int blk = (lane >> 4) & 1, q4 = (lane & 15) >> 2, p4 = lane & 3;
  const int xcd = blockIdx.x & 7, j = blockIdx.x >> 3;
  const int rt = 8 * (xcd & 3) + (j & 7), ct = 4 * (xcd >> 2) + (j >> 3);

  const size_t xo = (size_t)(rt * 64 + wm * 32 + r) * LD_X + (ct * 64 + wn * 32 + kh * 16 + hh * 8) * 2;
  const u32x4 xin = *(const GAS u32x4*)(p.x + xo);

  const i32x4 sa = make_srd(p.ws + WS_AFRAG, 64 * NKS * 1024);
  const i32x4 sd = make_srd(p.wd, 1792 * LD_W2), so = make_srd(p.wo, 512 * LD_W2);
  const unsigned va = lane * 16;
  const unsigned vw = (16 * v + (lane >> 3)) * LD_W2 + ((lane & 7) ^ (((lane >> 4) & 1) << 2)) * 16;
  const unsigned a0 = ((2 * rt + (v >> 1)) * NKS + 72 * kh + 2 * (v & 1)) * 1024;
  const unsigned lw = kh * 16384 + v * 2048;
  auto issue = [&](int i) {
    if (i >= 18) return;
    const unsigned l = (i & 3) * 32768 + lw;
    dma16(l, va, sa, a0 + i * 4096);
    dma16(l + 1024, va, sa, a0 + i * 4096 + 1024);
    const bool from_wo = i >= 10 && kh;
    const i32x4 s = from_wo ? so : sd;
    const unsigned o = ct * 128 + (from_wo ? (i - 10) * 64 : kh * 1152 + i * 64) * LD_W2;
    dma16(l + 8192, vw, s, o);
    dma16(l + 8192 + 1024, vw, s, o + 8 * LD_W2);
  };
  const unsigned ra = kh * 16384 + wm * 4096 + lane * 16;
  const unsigned rw = kh * 16384 + 8192 + (4 * hh + q4) * 128 + ((wn ^ (q4 >> 1)) * 64) + 32 * blk + 8 * p4;

  issue(0); issue(1); issue(2);
  wait_vm<8>();
  wg_barrier();
  issue(3);
  f32x16 acc = {}, accb = {};
  u32x4 fa[2], fw[2];
  fa[0] = lds_r128(ra);
  fw[0] = lds_tr2(rw, rw + 1024);
#pragma unroll
  for (int idx = 0; idx < 72; ++idx) {
    const int i = idx / 4, s = idx & 3;
    if (s == 3 && i < 17) {
      if (i < 15) wait_vm<8>(); else if (i == 15) wait_vm<4>(); else wait_vm<0>();
      wg_barrier();
      issue(i + 4);
    }
    if (idx + 1 < 72) {
      const unsigned o = (((idx + 1) / 4) & 3) * 32768, sn = (idx + 1) & 3;
      fa[(idx + 1) & 1] = lds_r128(ra + o + sn * 1024);
      fw[(idx + 1) & 1] = lds_tr2(rw + o + sn * 2048, rw + o + sn * 2048 + 1024);
    }
    if (idx == 40 && kh) { accb = acc; acc = f32x16{}; }
    acc = mfma32(fw[idx & 1], fa[idx & 1], acc);
  }
  const unsigned ex = v * 6144 + lane * 16;
  float b[8], sv[8];
  if (kh) {
    lds_w128(ex, __builtin_bit_cast(u32x4, f32x4{accb[0], accb[1], accb[2], accb[3]}));
    lds_w128(ex + 1024, __builtin_bit_cast(u32x4, f32x4{accb[4], accb[5], accb[6], accb[7]}));
    lds_w128(ex + 2048, __builtin_bit_cast(u32x4, f32x4{acc[0], acc[1], acc[2], acc[3]}));
    lds_w128(ex + 3072, __builtin_bit_cast(u32x4, f32x4{acc[4], acc[5], acc[6], acc[7]}));
  } else {
    lds_w128(ex + 4096, __builtin_bit_cast(u32x4, f32x4{acc[8], acc[9], acc[10], acc[11]}));
    lds_w128(ex + 5120, __builtin_bit_cast(u32x4, f32x4{acc[12], acc[13], acc[14], acc[15]}));
  }
  wg_barrier();
  if (kh) {
    const f32x4 t0 = __builtin_bit_cast(f32x4, lds_r128(ex + 4096)), t1 = __builtin_bit_cast(f32x4, lds_r128(ex + 5120));
#pragma unroll
    for (int e = 0; e < 4; ++e) {
      b[e] = accb[8 + e] + t0[e]; b[4 + e] = accb[12 + e] + t1[e];
      sv[e] = acc[8 + e]; sv[4 + e] = acc[12 + e];
    }
  } else {
    const f32x4 t0 = __builtin_bit_cast(f32x4, lds_r128(ex)), t1 = __builtin_bit_cast(f32x4, lds_r128(ex + 1024));
    const f32x4 t2 = __builtin_bit_cast(f32x4, lds_r128(ex + 2048)), t3 = __builtin_bit_cast(f32x4, lds_r128(ex + 3072));
#pragma unroll
    for (int e = 0; e < 4; ++e) {
      b[e] = acc[e] + t0[e]; b[4 + e] = acc[4 + e] + t1[e];
      sv[e] = t2[e]; sv[4 + e] = t3[e];
    }
  }
  float y[8];
#pragma unroll
  for (int e = 0; e < 8; ++e) {
    const float sg = __builtin_amdgcn_rcpf(1.f + __builtin_amdgcn_exp2f(b[e] * -1.44269504088896f));
    y[e] = sv[e] * (b[e] * sg);
  }
#pragma unroll
  for (int e = 0; e < 4; ++e) {
    const auto t = __builtin_amdgcn_permlane32_swap(asu(y[e]), asu(y[4 + e]), false, false);
    y[e] = asf(t[0]);
    y[4 + e] = asf(t[1]);
  }
  u32x4 o;
#pragma unroll
  for (int e = 0; e < 4; ++e) o[e] = pack2(bf_lo(xin[e]) + y[2 * e], bf_hi(xin[e]) + y[2 * e + 1]);
  *(GAS u32x4*)(p.out + xo) = o;
}

extern "C" void kernel_launch(void* const* d_in, const int* in_sizes, int n_in, void* d_out, int out_size,
                              void* d_ws, size_t ws_size, hipStream_t stream) {
  Args a;
  a.x = (const char*)d_in[0]; a.wqkv = (const char*)d_in[1]; a.wphi = (const char*)d_in[2]; a.wo = (const char*)d_in[3];
  a.wg = (const char*)d_in[5]; a.wu = (const char*)d_in[6]; a.wd = (const char*)d_in[7];
  a.ws = (char*)d_ws; a.out = (char*)d_out;
  (void)hipFuncSetAttribute((const void*)hh_proj_kernel, hipFuncAttributeMaxDynamicSharedMemorySize, LDS1);
  (void)hipFuncSetAttribute((const void*)hh_out_kernel, hipFuncAttributeMaxDynamicSharedMemorySize, LDS2);
  hh_proj_kernel<<<256, 512, LDS1, stream>>>(a);
  hh_out_kernel<<<256, 512, LDS2, stream>>>(a);
}
```

```cpp
#include <hip/hip_runtime.h>
#include <hip/hip_bf16.h>
#include <stdint.h>

typedef unsigned long long u64;
typedef __attribute__((ext_vector_type(8))) __bf16 b16x8;
typedef __attribute__((ext_vector_type(2))) __bf16 b16x2;
typedef __attribute__((ext_vector_type(4))) short s16x4;
typedef __attribute__((ext_vector_type(2))) float f32x2;
typedef __attribute__((ext_vector_type(4))) float f32x4;
typedef __attribute__((ext_vector_type(16))) float f32x16;
typedef __attribute__((ext_vector_type(2))) unsigned u32x2;
typedef __attribute__((ext_vector_type(4))) unsigned u32x4;
typedef __attribute__((ext_vector_type(4))) int i32x4;
#define LAS __attribute__((address_space(3)))
#define GAS __attribute__((address_space(1)))
#define DI __device__ __forceinline__

constexpr int LD_X = 1024, LD_QKV = 3072, LD_GU = 3584, LD_W2 = 1024;
constexpr int NKS = 144;
constexpr size_t WS_AFRAG = 0;
constexpr size_t WS_KVP = 64ull * NKS * 1024;
constexpr int KVP_BYTES = 4608;
constexpr size_t WS_FLAG = WS_KVP + 8 * 16 * KVP_BYTES;
constexpr size_t WS_EPOCH = WS_FLAG + 8 * 128;

constexpr int L_X = 0;
constexpr int L_PK = 0;
constexpr int L_V = 8 * 4352;
constexpr int L_PHI = 53248;
constexpr int L_CS = 55296;
constexpr int L_ST = 58368;
constexpr int L_KVT = 60416;
constexpr int L_W = 65536;
constexpr int LDS1 = 163840;
constexpr int LDS2 = 131072;

#define STAMP(k, n)

struct Args {
  const char* x; const char* wqkv; const char* wphi; const char* wo;
  const char* wg; const char* wu; const char* wd;
  char* ws; char* out;
};

DI i32x4 make_srd(const char* p, unsigned bytes) {
  const u64 a = (u64)p;
  i32x4 r;
  r[0] = __builtin_amdgcn_readfirstlane((int)(unsigned)a);
  r[1] = __builtin_amdgcn_readfirstlane((int)(unsigned)(a >> 32) & 0xffff);
  r[2] = (int)bytes;
  r[3] = 0x00020000;
  return r;
}
DI void dma16(unsigned lds, unsigned voff, const i32x4& srd, unsigned soff) {
  asm volatile("s_mov_b32 m0, %0\n\ts_nop 0\n\tbuffer_load_dwordx4 %1, %2, %3 offen lds"
               :: "s"(lds), "v"(voff), "s"(srd), "s"(soff) : "memory");
}
DI void dma4(unsigned lds, unsigned voff, const i32x4& srd, unsigned soff) {
  asm volatile("s_mov_b32 m0, %0\n\ts_nop 0\n\tbuffer_load_dword %1, %2, %3 offen lds"
               :: "s"(lds), "v"(voff), "s"(srd), "s"(soff) : "memory");
}
DI void dma16g(unsigned lds, const char* gptr) {
  asm volatile("s_mov_b32 m0, %0\n\ts_nop 0\n\tglobal_load_lds_dwordx4 %1, off" :: "s"(lds), "v"(gptr) : "memory");
}
template <int N> DI void wait_vm() { asm volatile("s_waitcnt vmcnt(%0)" :: "n"(N) : "memory"); }
DI void wg_barrier() {
  asm volatile("s_waitcnt lgkmcnt(0)" ::: "memory");
  __builtin_amdgcn_s_barrier();
  asm volatile("" ::: "memory");
}
DI u32x4 lds_r128(unsigned a) { return *(const LAS u32x4*)a; }
DI void lds_w128(unsigned a, u32x4 v) { *(LAS u32x4*)a = v; }
DI u32x4 lds_tr2(unsigned a0, unsigned a1) {
  const s16x4 lo = __builtin_amdgcn_ds_read_tr16_b64_v4i16((LAS s16x4*)a0);
  const s16x4 hi = __builtin_amdgcn_ds_read_tr16_b64_v4i16((LAS s16x4*)a1);
  const u32x2 l = __builtin_bit_cast(u32x2, lo), h = __builtin_bit_cast(u32x2, hi);
  return u32x4{l[0], l[1], h[0], h[1]};
}
DI f32x16 mfma32(u32x4 a, u32x4 b, f32x16 c) {
  return __builtin_amdgcn_mfma_f32_32x32x16_bf16(__builtin_bit_cast(b16x8, a), __builtin_bit_cast(b16x8, b), c, 0, 0, 0);
}
DI f32x4 mfma16(u32x4 a, u32x4 b, f32x4 c) {
  return __builtin_amdgcn_mfma_f32_16x16x32_bf16(__builtin_bit_cast(b16x8, a), __builtin_bit_cast(b16x8, b), c, 0, 0, 0);
}
DI float dot2(unsigned a, unsigned b, float c) {
  return __builtin_amdgcn_fdot2_f32_bf16(__builtin_bit_cast(b16x2, a), __builtin_bit_cast(b16x2, b), c, false);
}
DI unsigned pack2(float lo, float hi) {
  const b16x2 v = __builtin_convertvector(f32x2{lo, hi}, b16x2);
  return __builtin_bit_cast(unsigned, v);
}
DI float bf_lo(unsigned v) { return __builtin_bit_cast(float, v << 16); }
DI float bf_hi(unsigned v) { return __builtin_bit_cast(float, v & 0xffff0000u); }
DI float asf(unsigned v) { return __builtin_bit_cast(float, v); }
DI unsigned asu(float v) { return __builtin_bit_cast(unsigned, v); }
DI float half_sum(float v) {
  const auto r = __builtin_amdgcn_permlane32_swap(asu(v), asu(v), false, false);
  return asf(r[0]) + asf(r[1]);
}
DI float half_max(float v) {
  const auto r = __builtin_amdgcn_permlane32_swap(asu(v), asu(v), false, false);
  return fmaxf(asf(r[0]), asf(r[1]));
}
template <int CTRL> DI float dpp(float v) {
  return asf((unsigned)__builtin_amdgcn_update_dpp(0, (int)asu(v), CTRL, 0xf, 0xf, true));
}
DI u32x4 pack8(const f32x16& a, int o, float s) {
  return u32x4{pack2(a[o] * s, a[o + 1] * s), pack2(a[o + 2] * s, a[o + 3] * s),
               pack2(a[o + 4] * s, a[o + 5] * s), pack2(a[o + 6] * s, a[o + 7] * s)};
}
constexpr unsigned ONES2 = 0x3f803f80u;

DI void dual_softmax(const f32x16& p, int o, u32x4& pos, u32x4& neg) {
  const float L2E = 1.44269504088896f;
  float mx = p[o], mn = p[o];
#pragma unroll
  for (int i = 1; i < 8; ++i) { mx = fmaxf(mx, p[o + i]); mn = fminf(mn, p[o + i]); }
  mx = half_max(mx);
  mn = -half_max(-mn);
  const float mxl = mx * L2E, mnl = mn * L2E;
  float ep[8], en[8], sp = 0.f, sn = 0.f;
#pragma unroll
  for (int i = 0; i < 8; ++i) {
    ep[i] = __builtin_amdgcn_exp2f(fmaf(p[o + i], L2E, -mxl));
    en[i] = __builtin_amdgcn_exp2f(fmaf(p[o + i], -L2E, mnl));
    sp += ep[i]; sn += en[i];
  }
  const float rp = __builtin_amdgcn_rcpf(half_sum(sp)), rn = __builtin_amdgcn_rcpf(half_sum(sn));
#pragma unroll
  for (int i = 0; i < 4; ++i) {
    pos[i] = pack2(ep[2 * i] * rp, ep[2 * i + 1] * rp);
    neg[i] = pack2(en[2 * i] * rn, en[2 * i + 1] * rn);
  }
}

template <bool A> constexpr int n_units(int g) { return g < 8 ? 3 : g < 16 ? (A ? 1 : 3) : 0; }
constexpr int n_x(int j) { return j < 8 ? 2 : 0; }
template <bool A> constexpr int vm_at(int g) {
  return g < 8 ? n_units<A>(g + 1) + n_x(g + 1) + n_units<A>(g + 2) : n_units<A>(g + 1) + n_units<A>(g + 2);
}

struct K1 {
  i32x4 sx, s0, s1;
  unsigned vx0, vx1, vw;
  const char* vmix;
  unsigned xrow;
  unsigned col;
  unsigned w1k;
  unsigned xa[4];
  unsigned wa[2][4];
};
#define SB() __builtin_amdgcn_sched_barrier(0)

DI void issue_x(const K1& c, int j, int n) {
  if (j < 8) dma16(L_X + (j % 3) * 16384 + 2 * c.w1k + n * 1024, n ? c.vx1 : c.vx0, c.sx, c.xrow + 128 * j);
}
template <bool A> DI void issue_w(K1& c, int g, int u) {
  if (u >= n_units<A>(g)) return;
  const unsigned l = L_W + (g & 3) * 24576 + u * 8192 + c.w1k;
  const unsigned k0 = (g & 7) * 64;
  if (A) {
    if (g < 8) {
      dma16(l, c.vw, c.s0, c.col + k0 * LD_QKV + (u == 0 ? 1024 : u == 1 ? 2048 : 0));
    } else {
      dma16g(l, c.vmix);
      c.vmix += 64 * LD_GU;
    }
  } else {
    const int n = (g < 8 ? 0 : 3) + u;
    dma16(l, c.vw, (n & 1) ? c.s1 : c.s0, c.col + k0 * LD_GU + (n >> 1) * 128);
  }
}
template <bool A> DI void refill(K1& c, int g, int n) {
  if (n < 2) issue_x(c, g + 2, n);
  else issue_w<A>(c, g + 3, n - 2);
}
template <bool A> DI void open_stage(int g) {
  switch (vm_at<A>(g)) {
    case 0: wait_vm<0>(); break;
    case 1: wait_vm<1>(); break;
    case 2: wait_vm<2>(); break;
    case 3: wait_vm<3>(); break;
    case 6: wait_vm<6>(); break;
    default: wait_vm<8>(); break;
  }
  wg_barrier();
}
DI u32x4 read_w(const K1& c, int g, int i, int u) {
  const unsigned a = c.wa[(g >> 1) & 1][i] + (g & 1) * 24576 + u * 8192;
  return lds_tr2(a, a + 512);
}

template <bool A, int NU, int G0, bool OPEN_NEXT>
DI void gemm_pass(K1& c, u32x4 (&xf)[32], f32x16 (&acc)[NU], float (&cs)[NU], float& sum, float& sq) {
  u32x4 wf[NU];
  if (G0 == 0) xf[0] = lds_r128(c.xa[0]);
#pragma unroll
  for (int u = 0; u < NU; ++u) {
    wf[u] = read_w(c, G0, 0, u);
    cs[u] = 0.f;
#pragma unroll
    for (int e = 0; e < 16; ++e) acc[u][e] = 0.f;
  }
  SB();
#pragma unroll
  for (int idx = 0; idx < 32; ++idx) {
    const int g = G0 + idx / 4, i = idx & 3;
    const int gn = G0 + (idx + 1) / 4, in = (idx + 1) & 3;
    const bool more = idx + 1 < 32;
#pragma unroll
    for (int u = 0; u < NU; ++u) {
      acc[u] = mfma32(wf[u], xf[idx], acc[u]);
      if (i == 0) {
#pragma unroll
        for (int e = 0; e < 4; ++e) cs[u] = dot2(wf[u][e], ONES2, cs[u]);
        asm volatile("" : "+v"(cs[u]));
      }
      SB();
      if (u == 0 && i == 3 && (more || OPEN_NEXT)) { open_stage<A>(g + 1); SB(); }
      if (more) {
        if (u == 0 && G0 == 0) xf[idx + 1] = lds_r128(c.xa[in] + (gn % 3) * 16384);
        wf[u] = read_w(c, gn, in, u);
      }
      if (G0 == 0 && (i == 1 || i == 2) && u < 2) {
#pragma unroll
        for (int e = 2 * u; e < 2 * u + 2; ++e) { sum = dot2(xf[idx][e], ONES2, sum); sq = dot2(xf[idx][e], xf[idx][e], sq); }
        asm volatile("" : "+v"(sum), "+v"(sq));
      }
      if (NU == 3) {
        if (i == 3 && u > 0 && (more || OPEN_NEXT)) refill<A>(c, g + 1, u - 1);
        if (i < 3 && u == 2) refill<A>(c, g, i + 2);
      } else {
        if (i == 0) refill<A>(c, g, 2);
      }
      SB();
    }
  }
}

template <int NU> DI void put_colsum(const float (&cs)[NU], int wm, int wn, int r) {
#pragma unroll
  for (int u = 0; u < NU; ++u) {
    const float t = half_sum(cs[u]);
    const float hi = bf_lo(pack2(t, 0.f));
    *(LAS unsigned*)(L_CS + ((u * 2 + wn) * 32 + r) * 16 + wm * 4) = pack2(hi, t - hi);
  }
}
template <int NU> DI void apply_mean(f32x16 (&acc)[NU], u32x4 negm, int wn, int r) {
#pragma unroll
  for (int u = 0; u < NU; ++u) acc[u] = mfma32(lds_r128(L_CS + ((u * 2 + wn) * 32 + r) * 16), negm, acc[u]);
}
DI void store_frag(char* ws, int tb, int ks, int lane, u32x4 v) {
  *(GAS u32x4*)(ws + WS_AFRAG + ((size_t)(tb * NKS + ks) * 64 + lane) * 16) = v;
}
DI void store_h(char* ws, int tb, int ks, int lane, const f32x16& g, const f32x16& u, float r2) {
  f32x16 h;
#pragma unroll
  for (int e = 0; e < 16; ++e) h[e] = g[e] * u[e];
  store_frag(ws, tb, ks, lane, pack8(h, 0, r2));
  store_frag(ws, tb, ks + 1, lane, pack8(h, 8, r2));
}

template <bool A>
DI void k1_body(const Args& p, int rt, int ct) {
  const int tid = threadIdx.x, lane = tid & 63, w = __builtin_amdgcn_readfirstlane(tid >> 6);
  const int wm = w & 3, wn = w >> 2, r = lane & 31, hh = lane >> 5;
  const int blk = (lane >> 4) & 1, q4 = (lane & 15) >> 2, p4 = lane & 3;
  const int tb = rt * 4 + wm;

  STAMP(0, 0);
  u64 epoch = 0;
  GAS u64* const flags = (GAS u64*)(p.ws + WS_FLAG) + ct * 16;
  GAS u64* const epoch_word = (GAS u64*)(p.ws + WS_EPOCH) + ct * 16;
  if (A) epoch = __hip_atomic_load(epoch_word, __ATOMIC_RELAXED, __HIP_MEMORY_SCOPE_AGENT);

  K1 c;
  c.sx = make_srd(p.x, 2048 * LD_X);
  c.w1k = w * 1024;
  c.xrow = rt * 128 * LD_X;
  {
    const int row = 16 * w + (lane >> 3), ch = (lane & 7) ^ ((row >> 1) & 7);
    c.vx0 = row * LD_X + ch * 16;
    c.vx1 = (row + 8) * LD_X + (ch ^ 4) * 16;
    const int wrow = 8 * w + (lane >> 3), wch = (lane & 7) ^ (((lane >> 4) & 1) << 2);
    if (A) {
      c.s0 = make_srd(p.wqkv, 512 * LD_QKV); c.s1 = c.s0;
      c.vw = wrow * LD_QKV + wch * 16;
      c.col = ct * 128;
      c.vmix = (((wch >> 1) & 1) ? p.wu : p.wg) + wrow * LD_GU + (1536 + 32 * ct + 16 * (wch >> 2) + 8 * (wch & 1)) * 2;
    } else {
      c.s0 = make_srd(p.wg, 512 * LD_GU); c.s1 = make_srd(p.wu, 512 * LD_GU);
      c.vw = wrow * LD_GU + wch * 16;
      c.col = ct * 384;
      c.vmix = nullptr;
    }
    const int rot = wm + 2 * wn;
#pragma unroll
    for (int i = 0; i < 4; ++i) {
      const int s = (i + rot) & 3;
      c.xa[i] = L_X + (32 * wm + r) * 128 + (((2 * s + hh) ^ ((r >> 1) & 7)) * 16);
      c.wa[0][i] = L_W + 2048 * s + (8 * hh + q4) * 128 + ((wn ^ (q4 >> 1)) * 64) + 32 * blk + 8 * p4;
      c.wa[1][i] = c.wa[0][i] + 49152;
      asm volatile("" : "+v"(c.wa[0][i]), "+v"(c.wa[1][i]));
    }
  }
  if (A) dma4(L_PHI + w * 256, tid * 4, make_srd(p.wphi, 32 * 512), ct * 2048);
#pragma unroll
  for (int g = 0; g < 3; ++g) {
#pragma unroll
    for (int u = 0; u < 3; ++u) issue_w<A>(c, g, u);
    if (g < 2) { issue_x(c, g, 0); issue_x(c, g, 1); }
  }
  open_stage<A>(0);
  refill<A>(c, 0, 0); refill<A>(c, 0, 1);
  STAMP(0, 1);

  u32x4 xf[32];
  f32x16 acc[3];
  float cs[3], sum = 0.f, sq = 0.f;
  gemm_pass<A, 3, 0, true>(c, xf, acc, cs, sum, sq);

  STAMP(0, 2);
  put_colsum<3>(cs, wm, wn, r);
  sum = half_sum(sum); sq = half_sum(sq);
  *(LAS f32x2*)(L_ST + ((wm * 2 + wn) * 32 + r) * 8) = f32x2{sum, sq};
  wg_barrier();
  float rstd;
  u32x4 negm;
  {
    const f32x2 o = *(const LAS f32x2*)(L_ST + ((wm * 2 + (wn ^ 1)) * 32 + r) * 8);
    const float mean = (sum + o[0]) * (1.f / 512.f);
    const float var = fmaxf((sq + o[1]) * (1.f / 512.f) - mean * mean, 0.f);
    rstd = __builtin_amdgcn_rsqf(var + 1e-5f);
    const float mhi = bf_lo(pack2(-mean, 0.f));
    const float mm = hh ? (-mean - mhi) : mhi;
    const unsigned m2 = pack2(mm, mm);
    negm = u32x4{m2, m2, m2, m2};
  }
  apply_mean<3>(acc, negm, wn, r);
  const float r2 = rstd * rstd;

  if constexpr (!A) {
    const int ks0 = 12 * ct + 2 * wn;
    store_h(p.ws, tb, ks0, lane, acc[0], acc[1], r2);
    STAMP(0, 3);
    const f32x16 g1 = acc[2];
    float dummy0 = 0.f, dummy1 = 0.f;
    gemm_pass<A, 3, 8, false>(c, xf, acc, cs, dummy0, dummy1);
    STAMP(0, 6);
    put_colsum<3>(cs, wm, wn, r);
    wg_barrier();
    apply_mean<3>(acc, negm, wn, r);
    store_h(p.ws, tb, ks0 + 4, lane, g1, acc[0], r2);
    store_h(p.ws, tb, ks0 + 8, lane, acc[1], acc[2], r2);
    STAMP(0, 9);
  } else {
    u32x4 ph0, ph1;
    {
      const unsigned a = L_PHI + (2 * wn + blk) * 512 + (4 * hh + q4) * 32 + 8 * p4;
      const u32x4 wp = lds_tr2(a, a + 256);
      const u32x4 z = {0u, 0u, 0u, 0u};
      ph0 = blk ? z : wp;
      ph1 = blk ? wp : z;
    }
    const f32x16 zero16 = {};
    const unsigned img = hh * 2176 + (32 * wm + r) * 16;
    {
      f32x16 pk = mfma32(ph0, pack8(acc[0], 0, rstd), zero16);
      pk = mfma32(ph1, pack8(acc[0], 8, rstd), pk);
#pragma unroll
      for (int s = 0; s < 2; ++s) {
        u32x4 pos, neg;
        dual_softmax(pk, 8 * s, pos, neg);
        lds_w128(L_PK + (2 * wn + s) * 4352 + img, pos);
        lds_w128(L_PK + (4 + 2 * wn + s) * 4352 + img, neg);
        lds_w128(L_V + (2 * wn + s) * 4352 + img, pack8(acc[1], 8 * s, rstd));
      }
    }
    wg_barrier();
    STAMP(0, 3);
    char* const kvp = p.ws + WS_KVP + (size_t)ct * 16 * KVP_BYTES;
    const __amdgpu_buffer_rsrc_t rkv = __builtin_amdgcn_make_buffer_rsrc(kvp, 0, 16 * KVP_BYTES, 0x00020000);
    {
      const unsigned t = (p4 >> 1) * 2176 + (4 * (lane >> 4) + q4) * 16 + (p4 & 1) * 8;
      const u32x4 ones = {ONES2, ONES2, ONES2, ONES2};
      f32x4 kv = {}, z = {};
#pragma unroll
      for (int ks = 0; ks < 4; ++ks) {
        const unsigned ap = L_PK + w * 4352 + t + ks * 512, av = L_V + (w & 3) * 4352 + t + ks * 512;
        const u32x4 fa = lds_tr2(ap, ap + 256), fb = lds_tr2(av, av + 256);
        kv = mfma16(fa, fb, kv);
        z = mfma16(fa, ones, z);
      }
      __builtin_amdgcn_raw_buffer_store_b64(u32x2{pack2(kv[0], kv[1]), pack2(kv[2], kv[3])}, rkv,
                                            rt * KVP_BYTES + tid * 8, 0, 16);
      if ((lane & 15) == 0)
        __builtin_amdgcn_raw_buffer_store_b128(__builtin_bit_cast(u32x4, z), rkv,
                                               rt * KVP_BYTES + 4096 + w * 64 + (lane >> 4) * 16, 0, 16);
    }
    u32x4 fq[2][2];
    {
      f32x16 pq = mfma32(ph0, pack8(acc[2], 0, rstd), zero16);
      pq = mfma32(ph1, pack8(acc[2], 8, rstd), pq);
      dual_softmax(pq, 0, fq[0][0], fq[0][1]);
      dual_softmax(pq, 8, fq[1][0], fq[1][1]);
    }
    STAMP(0, 4);
    epoch += 1;
    wait_vm<0>();
    wg_barrier();
    if (tid == 0) __hip_atomic_store(flags + rt, epoch, __ATOMIC_RELAXED, __HIP_MEMORY_SCOPE_AGENT);

    STAMP(0, 5);
    f32x16 am[1];
    float csm[1], dummy0 = 0.f, dummy1 = 0.f;
    gemm_pass<A, 1, 8, false>(c, xf, am, csm, dummy0, dummy1);
    STAMP(0, 6);
    put_colsum<1>(csm, wm, wn, r);
    if (w == 0) {
      for (unsigned spin = 0; spin < (1u << 22); ++spin) {
        const u64 f = __hip_atomic_load(flags + (lane & 15), __ATOMIC_RELAXED, __HIP_MEMORY_SCOPE_AGENT);
        if (__all(f == epoch)) break;
        __builtin_amdgcn_s_sleep(1);
      }
      __builtin_amdgcn_fence(__ATOMIC_ACQUIRE, "wavefront");
      if (rt == 0 && lane == 0) __hip_atomic_store(epoch_word, epoch, __ATOMIC_RELAXED, __HIP_MEMORY_SCOPE_AGENT);
    }
    wg_barrier();
    STAMP(0, 7);
    u32x4 part[8];
#pragma unroll
    for (int i = 0; i < 8; ++i)
      part[i] = __builtin_amdgcn_raw_buffer_load_b128(rkv, (2 * i + (lane & 1)) * KVP_BYTES + (tid >> 1) * 16, 0, 16);
    const u32x4 zpart = __builtin_amdgcn_raw_buffer_load_b128(rkv, (lane & 15) * KVP_BYTES + 4096 + w * 64 + (lane >> 4) * 16, 0, 16);

    apply_mean<1>(am, negm, wn, r);
    {
      f32x16 h;
#pragma unroll
      for (int e = 0; e < 8; ++e) h[e] = am[0][e] * am[0][8 + e];
      store_frag(p.ws, tb, 96 + 2 * ct + wn, lane, pack8(h, 0, r2));
    }
    {
      float v[8] = {};
#pragma unroll
      for (int i = 0; i < 8; ++i)
#pragma unroll
        for (int e = 0; e < 4; ++e) { v[2 * e] += bf_lo(part[i][e]); v[2 * e + 1] += bf_hi(part[i][e]); }
      float t[4];
#pragma unroll
      for (int e = 0; e < 4; ++e) {
        const float give = (lane & 1) ? v[e] : v[4 + e], keep = (lane & 1) ? v[4 + e] : v[e];
        t[e] = keep + dpp<0xB1>(give);
      }
      const int dl = lane & 15, d = 8 * ((dl & 7) >> 2) + 4 * (dl >> 3) + (dl & 3);
      *(LAS u32x2*)(L_KVT + w * 544 + d * 32 + (lane >> 4) * 8) = u32x2{pack2(t[0], t[1]), pack2(t[2], t[3])};
      f32x4 z = __builtin_bit_cast(f32x4, zpart);
#pragma unroll
      for (int e = 0; e < 4; ++e) {
        float s = z[e];
        s += dpp<0xB1>(s); s += dpp<0x4E>(s); s += dpp<0x141>(s); s += dpp<0x140>(s);
        z[e] = s;
      }
      if (dl == 0) *(LAS u32x2*)(L_KVT + w * 544 + 512 + (lane >> 4) * 8) = u32x2{pack2(z[0], z[1]), pack2(z[2], z[3])};
    }
    wg_barrier();
    STAMP(0, 8);
#pragma unroll
    for (int s = 0; s < 2; ++s) {
      const unsigned a = L_KVT + (2 * wn + s) * 544 + (r < 16 ? r : 16) * 32 + 16 * hh;
      f32x16 o = mfma32(lds_r128(a), fq[s][0], zero16);
      o = mfma32(lds_r128(a + 4 * 544), fq[s][1], o);
      store_frag(p.ws, tb, 112 + 4 * ct + 2 * wn + s, lane, pack8(o, 0, __builtin_amdgcn_rcpf(o[8] + 1e-6f)));
    }
    STAMP(0, 9);
  }
}

__global__ __launch_bounds__(512, 2) void hh_proj_kernel(Args p) {
  extern __shared__ __attribute__((aligned(16))) char smem[];
  const int xcd = blockIdx.x & 7, j = blockIdx.x >> 3;
  const int rt = 4 * (xcd & 3) + (j & 3), ct = 4 * (xcd >> 2) + (j >> 3);
  if ((j >> 2) & 1) k1_body<true>(p, rt, ct);
  else k1_body<false>(p, rt, ct);
}

__global__ __launch_bounds__(512, 2) void hh_out_kernel(Args p) {
  extern __shared__ __attribute__((aligned(16))) char smem[];
  const int tid = threadIdx.x, lane = tid & 63, w = __builtin_amdgcn_readfirstlane(tid >> 6);
  const int kh = w >> 2, v = w & 3, wm = w & 1, wn = (w >> 1) & 1, r = lane & 31, hh = lane >> 5;
  const int blk = (lane >> 4) & 1, q4 = (lane & 15) >> 2, p4 = lane & 3;
  const int xcd = blockIdx.x & 7, j = blockIdx.x >> 3;
  const int rt = 8 * (xcd & 3) + (j & 7), ct = 4 * (xcd >> 2) + (j >> 3);

  STAMP(1, 0);
  const size_t xo = (size_t)(rt * 64 + wm * 32 + r) * LD_X + (ct * 64 + wn * 32 + kh * 16 + hh * 8) * 2;
  const u32x4 xin = *(const GAS u32x4*)(p.x + xo);

  const i32x4 sa = make_srd(p.ws + WS_AFRAG, 64 * NKS * 1024);
  const i32x4 sd = make_srd(p.wd, 1792 * LD_W2), so = make_srd(p.wo, 512 * LD_W2);
  const unsigned va = lane * 16;
  const unsigned vw = (16 * v + (lane >> 3)) * LD_W2 + ((lane & 7) ^ (((lane >> 4) & 1) << 2)) * 16;
  const unsigned a0 = ((2 * rt + (v >> 1)) * NKS + 72 * kh + 2 * (v & 1)) * 1024;
  const unsigned lw = kh * 16384 + v * 2048;
  auto issue = [&](int i) {
    if (i >= 18) return;
    const unsigned l = (i & 3) * 32768 + lw;
    dma16(l, va, sa, a0 + i * 4096);
    dma16(l + 1024, va, sa, a0 + i * 4096 + 1024);
    const bool from_wo = i >= 10 && kh;
    const i32x4 s = from_wo ? so : sd;
    const unsigned o = ct * 128 + (from_wo ? (i - 10) * 64 : kh * 1152 + i * 64) * LD_W2;
    dma16(l + 8192, vw, s, o);
    dma16(l + 8192 + 1024, vw, s, o + 8 * LD_W2);
  };
  const unsigned ra = kh * 16384 + wm * 4096 + lane * 16;
  const unsigned rw = kh * 16384 + 8192 + (4 * hh + q4) * 128 + ((wn ^ (q4 >> 1)) * 64) + 32 * blk + 8 * p4;

  issue(0); issue(1); issue(2);
  wait_vm<8>();
  wg_barrier();
  issue(3);
  STAMP(1, 1);
  f32x16 acc = {}, accb = {};
  u32x4 fa[2], fw[2];
  fa[0] = lds_r128(ra);
  fw[0] = lds_tr2(rw, rw + 1024);
#pragma unroll
  for (int idx = 0; idx < 72; ++idx) {
    const int i = idx / 4, s = idx & 3;
    if (s == 3 && i < 17) {
      if (i < 15) wait_vm<8>(); else if (i == 15) wait_vm<4>(); else wait_vm<0>();
      wg_barrier();
      issue(i + 4);
    }
    if (idx + 1 < 72) {
      const unsigned o = (((idx + 1) / 4) & 3) * 32768, sn = (idx + 1) & 3;
      fa[(idx + 1) & 1] = lds_r128(ra + o + sn * 1024);
      fw[(idx + 1) & 1] = lds_tr2(rw + o + sn * 2048, rw + o + sn * 2048 + 1024);
    }
    if (idx == 40 && kh) { accb = acc; acc = f32x16{}; }
    acc = mfma32(fw[idx & 1], fa[idx & 1], acc);
  }
  STAMP(1, 2);
  const unsigned ex = v * 6144 + lane * 16;
  float b[8], sv[8];
  if (kh) {
    lds_w128(ex, __builtin_bit_cast(u32x4, f32x4{accb[0], accb[1], accb[2], accb[3]}));
    lds_w128(ex + 1024, __builtin_bit_cast(u32x4, f32x4{accb[4], accb[5], accb[6], accb[7]}));
    lds_w128(ex + 2048, __builtin_bit_cast(u32x4, f32x4{acc[0], acc[1], acc[2], acc[3]}));
    lds_w128(ex + 3072, __builtin_bit_cast(u32x4, f32x4{acc[4], acc[5], acc[6], acc[7]}));
  } else {
    lds_w128(ex + 4096, __builtin_bit_cast(u32x4, f32x4{acc[8], acc[9], acc[10], acc[11]}));
    lds_w128(ex + 5120, __builtin_bit_cast(u32x4, f32x4{acc[12], acc[13], acc[14], acc[15]}));
  }
  wg_barrier();
  if (kh) {
    const f32x4 t0 = __builtin_bit_cast(f32x4, lds_r128(ex + 4096)), t1 = __builtin_bit_cast(f32x4, lds_r128(ex + 5120));
#pragma unroll
    for (int e = 0; e < 4; ++e) {
      b[e] = accb[8 + e] + t0[e]; b[4 + e] = accb[12 + e] + t1[e];
      sv[e] = acc[8 + e]; sv[4 + e] = acc[12 + e];
    }
  } else {
    const f32x4 t0 = __builtin_bit_cast(f32x4, lds_r128(ex)), t1 = __builtin_bit_cast(f32x4, lds_r128(ex + 1024));
    const f32x4 t2 = __builtin_bit_cast(f32x4, lds_r128(ex + 2048)), t3 = __builtin_bit_cast(f32x4, lds_r128(ex + 3072));
#pragma unroll
    for (int e = 0; e < 4; ++e) {
      b[e] = acc[e] + t0[e]; b[4 + e] = acc[4 + e] + t1[e];
      sv[e] = t2[e]; sv[4 + e] = t3[e];
    }
  }
  float y[8];
#pragma unroll
  for (int e = 0; e < 8; ++e) {
    const float sg = __builtin_amdgcn_rcpf(1.f + __builtin_amdgcn_exp2f(b[e] * -1.44269504088896f));
    y[e] = sv[e] * (b[e] * sg);
  }
#pragma unroll
  for (int e = 0; e < 4; ++e) {
    const auto t = __builtin_amdgcn_permlane32_swap(asu(y[e]), asu(y[4 + e]), false, false);
    y[e] = asf(t[0]);
    y[4 + e] = asf(t[1]);
  }
  u32x4 o;
#pragma unroll
  for (int e = 0; e < 4; ++e) o[e] = pack2(bf_lo(xin[e]) + y[2 * e], bf_hi(xin[e]) + y[2 * e + 1]);
  *(GAS u32x4*)(p.out + xo) = o;
  STAMP(1, 3);
}

__global__ __launch_bounds__(512) void pre_kernel(Args a, int touch_ws) {
  const int t = blockIdx.x * 512 + threadIdx.x;
  const u32x4* p[6] = {(const u32x4*)a.x, (const u32x4*)a.wqkv, (const u32x4*)a.wo, (const u32x4*)a.wg, (const u32x4*)a.wu, (const u32x4*)a.wd};
  const int n16[6] = {131072, 98304, 32768, 114688, 114688, 114688};
  u32x4 s = {};
#pragma unroll
  for (int i = 0; i < 6; ++i) if (t < n16[i]) s ^= p[i][t];
  if (touch_ws) {
#pragma unroll
    for (int i = 0; i < 5; ++i) if (t + i * 131072 < 64 * 144 * 64) ((u32x4*)a.ws)[t + i * 131072] = s;
  }
  asm volatile("" :: "v"(s));
}
__global__ void empty_kernel() {}
extern "C" void kernel_launch(void* const* d_in, const int* in_sizes, int n_in, void* d_out, int out_size,
                              void* d_ws, size_t ws_size, hipStream_t stream) {
  Args a;
  a.x = (const char*)d_in[0]; a.wqkv = (const char*)d_in[1]; a.wphi = (const char*)d_in[2]; a.wo = (const char*)d_in[3];
  a.wg = (const char*)d_in[5]; a.wu = (const char*)d_in[6]; a.wd = (const char*)d_in[7];
  a.ws = (char*)d_ws; a.out = (char*)d_out;
  (void)hipFuncSetAttribute((const void*)hh_proj_kernel, hipFuncAttributeMaxDynamicSharedMemorySize, LDS1);
  (void)hipFuncSetAttribute((const void*)hh_out_kernel, hipFuncAttributeMaxDynamicSharedMemorySize, LDS2);
  pre_kernel<<<256, 512, 0, stream>>>(a, 0);
  hh_proj_kernel<<<256, 512, LDS1, stream>>>(a);
  hh_out_kernel<<<256, 512, LDS2, stream>>>(a);
}
```

```cpp
#include <hip/hip_runtime.h>
#include <hip/hip_bf16.h>
#include <stdint.h>

typedef unsigned long long u64;
typedef __attribute__((ext_vector_type(8))) __bf16 b16x8;
typedef __attribute__((ext_vector_type(2))) __bf16 b16x2;
typedef __attribute__((ext_vector_type(4))) short s16x4;
typedef __attribute__((ext_vector_type(2))) float f32x2;
typedef __attribute__((ext_vector_type(4))) float f32x4;
typedef __attribute__((ext_vector_type(16))) float f32x16;
typedef __attribute__((ext_vector_type(2))) unsigned u32x2;
typedef __attribute__((ext_vector_type(4))) unsigned u32x4;
typedef __attribute__((ext_vector_type(4))) int i32x4;
#define LAS __attribute__((address_space(3)))
#define GAS __attribute__((address_space(1)))
#define DI __device__ __forceinline__

constexpr int LD_X = 1024, LD_QKV = 3072, LD_GU = 3584, LD_W2 = 1024;
constexpr int NKS = 144;
constexpr size_t WS_AFRAG = 0;
constexpr size_t WS_KVP = 64ull * NKS * 1024;
constexpr int KVP_BYTES = 4608;
constexpr size_t WS_FLAG = WS_KVP + 8 * 16 * KVP_BYTES;
constexpr size_t WS_EPOCH = WS_FLAG + 8 * 128;

constexpr int L_X = 0;
constexpr int L_PK = 0;
constexpr int L_V = 8 * 4352;
constexpr int L_PHI = 53248;
constexpr int L_CS = 55296;
constexpr int L_ST = 58368;
constexpr int L_KVT = 60416;
constexpr int L_W = 65536;
constexpr int LDS1 = 163840;
constexpr int LDS2 = 131072;

#define STAMP(k, n)

struct Args {
  const char* x; const char* wqkv; const char* wphi; const char* wo;
  const char* wg; const char* wu; const char* wd;
  char* ws; char* out;
};

DI i32x4 make_srd(const char* p, unsigned bytes) {
  const u64 a = (u64)p;
  i32x4 r;
  r[0] = __builtin_amdgcn_readfirstlane((int)(unsigned)a);
  r[1] = __builtin_amdgcn_readfirstlane((int)(unsigned)(a >> 32) & 0xffff);
  r[2] = (int)bytes;
  r[3] = 0x00020000;
  return r;
}
DI void dma16(unsigned lds, unsigned voff, const i32x4& srd, unsigned soff) {
  asm volatile("s_mov_b32 m0, %0\n\ts_nop 0\n\tbuffer_load_dwordx4 %1, %2, %3 offen lds"
               :: "s"(lds), "v"(voff), "s"(srd), "s"(soff) : "memory");
}
DI void dma4(unsigned lds, unsigned voff, const i32x4& srd, unsigned soff) {
  asm volatile("s_mov_b32 m0, %0\n\ts_nop 0\n\tbuffer_load_dword %1, %2, %3 offen lds"
               :: "s"(lds), "v"(voff), "s"(srd), "s"(soff) : "memory");
}
DI void dma16g(unsigned lds, const char* gptr) {
  asm volatile("s_mov_b32 m0, %0\n\ts_nop 0\n\tglobal_load_lds_dwordx4 %1, off" :: "s"(lds), "v"(gptr) : "memory");
}
template <int N> DI void wait_vm() { asm volatile("s_waitcnt vmcnt(%0)" :: "n"(N) : "memory"); }
DI void wg_barrier() {
  asm volatile("s_waitcnt lgkmcnt(0)" ::: "memory");
  __builtin_amdgcn_s_barrier();
  asm volatile("" ::: "memory");
}
DI u32x4 lds_r128(unsigned a) { return *(const LAS u32x4*)a; }
DI void lds_w128(unsigned a, u32x4 v) { *(LAS u32x4*)a = v; }
DI u32x4 lds_tr2(unsigned a0, unsigned a1) {
  const s16x4 lo = __builtin_amdgcn_ds_read_tr16_b64_v4i16((LAS s16x4*)a0);
  const s16x4 hi = __builtin_amdgcn_ds_read_tr16_b64_v4i16((LAS s16x4*)a1);
  const u32x2 l = __builtin_bit_cast(u32x2, lo), h = __builtin_bit_cast(u32x2, hi);
  return u32x4{l[0], l[1], h[0], h[1]};
}
DI f32x16 mfma32(u32x4 a, u32x4 b, f32x16 c) {
  return __builtin_amdgcn_mfma_f32_32x32x16_bf16(__builtin_bit_cast(b16x8, a), __builtin_bit_cast(b16x8, b), c, 0, 0, 0);
}
DI f32x4 mfma16(u32x4 a, u32x4 b, f32x4 c) {
  return __builtin_amdgcn_mfma_f32_16x16x32_bf16(__builtin_bit_cast(b16x8, a), __builtin_bit_cast(b16x8, b), c, 0, 0, 0);
}
DI float dot2(unsigned a, unsigned b, float c) {
  return __builtin_amdgcn_fdot2_f32_bf16(__builtin_bit_cast(b16x2, a), __builtin_bit_cast(b16x2, b), c, false);
}
DI unsigned pack2(float lo, float hi) {
  const b16x2 v = __builtin_convertvector(f32x2{lo, hi}, b16x2);
  return __builtin_bit_cast(unsigned, v);
}
DI float bf_lo(unsigned v) { return __builtin_bit_cast(float, v << 16); }
DI float bf_hi(unsigned v) { return __builtin_bit_cast(float, v & 0xffff0000u); }
DI float asf(unsigned v) { return __builtin_bit_cast(float, v); }
DI unsigned asu(float v) { return __builtin_bit_cast(unsigned, v); }
DI float half_sum(float v) {
  const auto r = __builtin_amdgcn_permlane32_swap(asu(v), asu(v), false, false);
  return asf(r[0]) + asf(r[1]);
}
DI float half_max(float v) {
  const auto r = __builtin_amdgcn_permlane32_swap(asu(v), asu(v), false, false);
  return fmaxf(asf(r[0]), asf(r[1]));
}
template <int CTRL> DI float dpp(float v) {
  return asf((unsigned)__builtin_amdgcn_update_dpp(0, (int)asu(v), CTRL, 0xf, 0xf, true));
}
DI u32x4 pack8(const f32x16& a, int o, float s) {
  return u32x4{pack2(a[o] * s, a[o + 1] * s), pack2(a[o + 2] * s, a[o + 3] * s),
               pack2(a[o + 4] * s, a[o + 5] * s), pack2(a[o + 6] * s, a[o + 7] * s)};
}
constexpr unsigned ONES2 = 0x3f803f80u;

DI void dual_softmax(const f32x16& p, int o, u32x4& pos, u32x4& neg) {
  const float L2E = 1.44269504088896f;
  float mx = p[o], mn = p[o];
#pragma unroll
  for (int i = 1; i < 8; ++i) { mx = fmaxf(mx, p[o + i]); mn = fminf(mn, p[o + i]); }
  mx = half_max(mx);
  mn = -half_max(-mn);
  const float mxl = mx * L2E, mnl = mn * L2E;
  float ep[8], en[8], sp = 0.f, sn = 0.f;
#pragma unroll
  for (int i = 0; i < 8; ++i) {
    ep[i] = __builtin_amdgcn_exp2f(fmaf(p[o + i], L2E, -mxl));
    en[i] = __builtin_amdgcn_exp2f(fmaf(p[o + i], -L2E, mnl));
    sp += ep[i]; sn += en[i];
  }
  const float rp = __builtin_amdgcn_rcpf(half_sum(sp)), rn = __builtin_amdgcn_rcpf(half_sum(sn));
#pragma unroll
  for (int i = 0; i < 4; ++i) {
    pos[i] = pack2(ep[2 * i] * rp, ep[2 * i + 1] * rp);
    neg[i] = pack2(en[2 * i] * rn, en[2 * i + 1] * rn);
  }
}

template <bool A> constexpr int n_units(int g) { return g < 8 ? 3 : g < 16 ? (A ? 1 : 3) : 0; }
constexpr int n_x(int j) { return j < 8 ? 2 : 0; }
template <bool A> constexpr int vm_at(int g) {
  return g < 8 ? n_units<A>(g + 1) + n_x(g + 1) + n_units<A>(g + 2) : n_units<A>(g + 1) + n_units<A>(g + 2);
}

struct K1 {
  i32x4 sx, s0, s1;
  unsigned vx0, vx1, vw;
  const char* vmix;
  unsigned xrow;
  unsigned col;
  unsigned w1k;
  unsigned xa[4];
  unsigned wa[2][4];
};
#define SB() __builtin_amdgcn_sched_barrier(0)

DI void issue_x(const K1& c, int j, int n) {
  if (j < 8) dma16(L_X + (j % 3) * 16384 + 2 * c.w1k + n * 1024, n ? c.vx1 : c.vx0, c.sx, c.xrow + 128 * j);
}
template <bool A> DI void issue_w(K1& c, int g, int u) {
  if (u >= n_units<A>(g)) return;
  const unsigned l = L_W + (g & 3) * 24576 + u * 8192 + c.w1k;
  const unsigned k0 = (g & 7) * 64;
  if (A) {
    if (g < 8) {
      dma16(l, c.vw, c.s0, c.col + k0 * LD_QKV + (u == 0 ? 1024 : u == 1 ? 2048 : 0));
    } else {
      dma16g(l, c.vmix);
      c.vmix += 64 * LD_GU;
    }
  } else {
    const int n = (g < 8 ? 0 : 3) + u;
    dma16(l, c.vw, (n & 1) ? c.s1 : c.s0, c.col + k0 * LD_GU + (n >> 1) * 128);
  }
}
template <bool A> DI void refill(K1& c, int g, int n) {
  if (n < 2) issue_x(c, g + 2, n);
  else issue_w<A>(c, g + 3, n - 2);
}
template <bool A> DI void open_stage(int g) {
  switch (vm_at<A>(g)) {
    case 0: wait_vm<0>(); break;
    case 1: wait_vm<1>(); break;
    case 2: wait_vm<2>(); break;
    case 3: wait_vm<3>(); break;
    case 6: wait_vm<6>(); break;
    default: wait_vm<8>(); break;
  }
  wg_barrier();
}
DI u32x4 read_w(const K1& c, int g, int i, int u) {
  const unsigned a = c.wa[(g >> 1) & 1][i] + (g & 1) * 24576 + u * 8192;
  return lds_tr2(a, a + 512);
}

template <bool A, int NU, int G0, bool OPEN_NEXT>
DI void gemm_pass(K1& c, u32x4 (&xf)[32], f32x16 (&acc)[NU], float (&cs)[NU], float& sum, float& sq) {
  u32x4 wf[NU];
  if (G0 == 0) xf[0] = lds_r128(c.xa[0]);
#pragma unroll
  for (int u = 0; u < NU; ++u) {
    wf[u] = read_w(c, G0, 0, u);
    cs[u] = 0.f;
#pragma unroll
    for (int e = 0; e < 16; ++e) acc[u][e] = 0.f;
  }
  SB();
#pragma unroll
  for (int idx = 0; idx < 32; ++idx) {
    const int g = G0 + idx / 4, i = idx & 3;
    const int gn = G0 + (idx + 1) / 4, in = (idx + 1) & 3;
    const bool more = idx + 1 < 32;
#pragma unroll
    for (int u = 0; u < NU; ++u) {
      acc[u] = mfma32(wf[u], xf[idx], acc[u]);
      if (i == 0) {
#pragma unroll
        for (int e = 0; e < 4; ++e) cs[u] = dot2(wf[u][e], ONES2, cs[u]);
        asm volatile("" : "+v"(cs[u]));
      }
      SB();
      if (u == 0 && i == 3 && (more || OPEN_NEXT)) { open_stage<A>(g + 1); SB(); }
      if (more) {
        if (u == 0 && G0 == 0) xf[idx + 1] = lds_r128(c.xa[in] + (gn % 3) * 16384);
        wf[u] = read_w(c, gn, in, u);
      }
      if (G0 == 0 && (i == 1 || i == 2) && u < 2) {
#pragma unroll
        for (int e = 2 * u; e < 2 * u + 2; ++e) { sum = dot2(xf[idx][e], ONES2, sum); sq = dot2(xf[idx][e], xf[idx][e], sq); }
        asm volatile("" : "+v"(sum), "+v"(sq));
      }
      if (NU == 3) {
        if (i == 3 && u > 0 && (more || OPEN_NEXT)) refill<A>(c, g + 1, u - 1);
        if (i < 3 && u == 2) refill<A>(c, g, i + 2);
      } else {
        if (i == 0) refill<A>(c, g, 2);
      }
      SB();
    }
  }
}

template <int NU> DI void put_colsum(const float (&cs)[NU], int wm, int wn, int r) {
#pragma unroll
  for (int u = 0; u < NU; ++u) {
    const float t = half_sum(cs[u]);
    const float hi = bf_lo(pack2(t, 0.f));
    *(LAS unsigned*)(L_CS + ((u * 2 + wn) * 32 + r) * 16 + wm * 4) = pack2(hi, t - hi);
  }
}
template <int NU> DI void apply_mean(f32x16 (&acc)[NU], u32x4 negm, int wn, int r) {
#pragma unroll
  for (int u = 0; u < NU; ++u) acc[u] = mfma32(lds_r128(L_CS + ((u * 2 + wn) * 32 + r) * 16), negm, acc[u]);
}
DI void store_frag(char* ws, int tb, int ks, int lane, u32x4 v) {
  *(GAS u32x4*)(ws + WS_AFRAG + ((size_t)(tb * NKS + ks) * 64 + lane) * 16) = v;
}
DI void store_h(char* ws, int tb, int ks, int lane, const f32x16& g, const f32x16& u, float r2) {
  f32x16 h;
#pragma unroll
  for (int e = 0; e < 16; ++e) h[e] = g[e] * u[e];
  store_frag(ws, tb, ks, lane, pack8(h, 0, r2));
  store_frag(ws, tb, ks + 1, lane, pack8(h, 8, r2));
}

template <bool A>
DI void k1_body(const Args& p, int rt, int ct) {
  const int tid = threadIdx.x, lane = tid & 63, w = __builtin_amdgcn_readfirstlane(tid >> 6);
  const int wm = w & 3, wn = w >> 2, r = lane & 31, hh = lane >> 5;
  const int blk = (lane >> 4) & 1, q4 = (lane & 15) >> 2, p4 = lane & 3;
  const int tb = rt * 4 + wm;

  STAMP(0, 0);
  u64 epoch = 0;
  GAS u64* const flags = (GAS u64*)(p.ws + WS_FLAG) + ct * 16;
  GAS u64* const epoch_word = (GAS u64*)(p.ws + WS_EPOCH) + ct * 16;
  if (A) epoch = __hip_atomic_load(epoch_word, __ATOMIC_RELAXED, __HIP_MEMORY_SCOPE_AGENT);

  K1 c;
  c.sx = make_srd(p.x, 2048 * LD_X);
  c.w1k = w * 1024;
  c.xrow = rt * 128 * LD_X;
  {
    const int row = 16 * w + (lane >> 3), ch = (lane & 7) ^ ((row >> 1) & 7);
    c.vx0 = row * LD_X + ch * 16;
    c.vx1 = (row + 8) * LD_X + (ch ^ 4) * 16;
    const int wrow = 8 * w + (lane >> 3), wch = (lane & 7) ^ (((lane >> 4) & 1) << 2);
    if (A) {
      c.s0 = make_srd(p.wqkv, 512 * LD_QKV); c.s1 = c.s0;
      c.vw = wrow * LD_QKV + wch * 16;
      c.col = ct * 128;
      c.vmix = (((wch >> 1) & 1) ? p.wu : p.wg) + wrow * LD_GU + (1536 + 32 * ct + 16 * (wch >> 2) + 8 * (wch & 1)) * 2;
    } else {
      c.s0 = make_srd(p.wg, 512 * LD_GU); c.s1 = make_srd(p.wu, 512 * LD_GU);
      c.vw = wrow * LD_GU + wch * 16;
      c.col = ct * 384;
      c.vmix = nullptr;
    }
    const int rot = wm + 2 * wn;
#pragma unroll
    for (int i = 0; i < 4; ++i) {
      const int s = (i + rot) & 3;
      c.xa[i] = L_X + (32 * wm + r) * 128 + (((2 * s + hh) ^ ((r >> 1) & 7)) * 16);
      c.wa[0][i] = L_W + 2048 * s + (8 * hh + q4) * 128 + ((wn ^ (q4 >> 1)) * 64) + 32 * blk + 8 * p4;
      c.wa[1][i] = c.wa[0][i] + 49152;
      asm volatile("" : "+v"(c.wa[0][i]), "+v"(c.wa[1][i]));
    }
  }
  if (A) dma4(L_PHI + w * 256, tid * 4, make_srd(p.wphi, 32 * 512), ct * 2048);
#pragma unroll
  for (int g = 0; g < 3; ++g) {
#pragma unroll
    for (int u = 0; u < 3; ++u) issue_w<A>(c, g, u);
    if (g < 2) { issue_x(c, g, 0); issue_x(c, g, 1); }
  }
  open_stage<A>(0);
  refill<A>(c, 0, 0); refill<A>(c, 0, 1);
  STAMP(0, 1);

  u32x4 xf[32];
  f32x16 acc[3];
  float cs[3], sum = 0.f, sq = 0.f;
  gemm_pass<A, 3, 0, true>(c, xf, acc, cs, sum, sq);

  STAMP(0, 2);
  put_colsum<3>(cs, wm, wn, r);
  sum = half_sum(sum); sq = half_sum(sq);
  *(LAS f32x2*)(L_ST + ((wm * 2 + wn) * 32 + r) * 8) = f32x2{sum, sq};
  wg_barrier();
  float rstd;
  u32x4 negm;
  {
    const f32x2 o = *(const LAS f32x2*)(L_ST + ((wm * 2 + (wn ^ 1)) * 32 + r) * 8);
    const float mean = (sum + o[0]) * (1.f / 512.f);
    const float var = fmaxf((sq + o[1]) * (1.f / 512.f) - mean * mean, 0.f);
    rstd = __builtin_amdgcn_rsqf(var + 1e-5f);
    const float mhi = bf_lo(pack2(-mean, 0.f));
    const float mm = hh ? (-mean - mhi) : mhi;
    const unsigned m2 = pack2(mm, mm);
    negm = u32x4{m2, m2, m2, m2};
  }
  apply_mean<3>(acc, negm, wn, r);
  const float r2 = rstd * rstd;

  if constexpr (!A) {
    const int ks0 = 12 * ct + 2 * wn;
    store_h(p.ws, tb, ks0, lane, acc[0], acc[1], r2);
    STAMP(0, 3);
    const f32x16 g1 = acc[2];
    float dummy0 = 0.f, dummy1 = 0.f;
    gemm_pass<A, 3, 8, false>(c, xf, acc, cs, dummy0, dummy1);
    STAMP(0, 6);
    put_colsum<3>(cs, wm, wn, r);
    wg_barrier();
    apply_mean<3>(acc, negm, wn, r);
    store_h(p.ws, tb, ks0 + 4, lane, g1, acc[0], r2);
    store_h(p.ws, tb, ks0 + 8, lane, acc[1], acc[2], r2);
    STAMP(0, 9);
  } else {
    u32x4 ph0, ph1;
    {
      const unsigned a = L_PHI + (2 * wn + blk) * 512 + (4 * hh + q4) * 32 + 8 * p4;
      const u32x4 wp = lds_tr2(a, a + 256);
      const u32x4 z = {0u, 0u, 0u, 0u};
      ph0 = blk ? z : wp;
      ph1 = blk ? wp : z;
    }
    const f32x16 zero16 = {};
    const unsigned img = hh * 2176 + (32 * wm + r) * 16;
    {
      f32x16 pk = mfma32(ph0, pack8(acc[0], 0, rstd), zero16);
      pk = mfma32(ph1, pack8(acc[0], 8, rstd), pk);
#pragma unroll
      for (int s = 0; s < 2; ++s) {
        u32x4 pos, neg;
        dual_softmax(pk, 8 * s, pos, neg);
        lds_w128(L_PK + (2 * wn + s) * 4352 + img, pos);
        lds_w128(L_PK + (4 + 2 * wn + s) * 4352 + img, neg);
        lds_w128(L_V + (2 * wn + s) * 4352 + img, pack8(acc[1], 8 * s, rstd));
      }
    }
    wg_barrier();
    STAMP(0, 3);
    char* const kvp = p.ws + WS_KVP + (size_t)ct * 16 * KVP_BYTES;
    const __amdgpu_buffer_rsrc_t rkv = __builtin_amdgcn_make_buffer_rsrc(kvp, 0, 16 * KVP_BYTES, 0x00020000);
    {
      const unsigned t = (p4 >> 1) * 2176 + (4 * (lane >> 4) + q4) * 16 + (p4 & 1) * 8;
      const u32x4 ones = {ONES2, ONES2, ONES2, ONES2};
      f32x4 kv = {}, z = {};
#pragma unroll
      for (int ks = 0; ks < 4; ++ks) {
        const unsigned ap = L_PK + w * 4352 + t + ks * 512, av = L_V + (w & 3) * 4352 + t + ks * 512;
        const u32x4 fa = lds_tr2(ap, ap + 256), fb = lds_tr2(av, av + 256);
        kv = mfma16(fa, fb, kv);
        z = mfma16(fa, ones, z);
      }
      __builtin_amdgcn_raw_buffer_store_b64(u32x2{pack2(kv[0], kv[1]), pack2(kv[2], kv[3])}, rkv,
                                            rt * KVP_BYTES + tid * 8, 0, 16);
      if ((lane & 15) == 0)
        __builtin_amdgcn_raw_buffer_store_b128(__builtin_bit_cast(u32x4, z), rkv,
                                               rt * KVP_BYTES + 4096 + w * 64 + (lane >> 4) * 16, 0, 16);
    }
    u32x4 fq[2][2];
    {
      f32x16 pq = mfma32(ph0, pack8(acc[2], 0, rstd), zero16);
      pq = mfma32(ph1, pack8(acc[2], 8, rstd), pq);
      dual_softmax(pq, 0, fq[0][0], fq[0][1]);
      dual_softmax(pq, 8, fq[1][0], fq[1][1]);
    }
    STAMP(0, 4);
    epoch += 1;
    wait_vm<0>();
    wg_barrier();
    if (tid == 0) __hip_atomic_store(flags + rt, epoch, __ATOMIC_RELAXED, __HIP_MEMORY_SCOPE_AGENT);

    STAMP(0, 5);
    f32x16 am[1];
    float csm[1], dummy0 = 0.f, dummy1 = 0.f;
    gemm_pass<A, 1, 8, false>(c, xf, am, csm, dummy0, dummy1);
    STAMP(0, 6);
    put_colsum<1>(csm, wm, wn, r);
    if (w == 0) {
      for (unsigned spin = 0; spin < (1u << 22); ++spin) {
        const u64 f = __hip_atomic_load(flags + (lane & 15), __ATOMIC_RELAXED, __HIP_MEMORY_SCOPE_AGENT);
        if (__all(f == epoch)) break;
        __builtin_amdgcn_s_sleep(1);
      }
      __builtin_amdgcn_fence(__ATOMIC_ACQUIRE, "wavefront");
      if (rt == 0 && lane == 0) __hip_atomic_store(epoch_word, epoch, __ATOMIC_RELAXED, __HIP_MEMORY_SCOPE_AGENT);
    }
    wg_barrier();
    STAMP(0, 7);
    u32x4 part[8];
#pragma unroll
    for (int i = 0; i < 8; ++i)
      part[i] = __builtin_amdgcn_raw_buffer_load_b128(rkv, (2 * i + (lane & 1)) * KVP_BYTES + (tid >> 1) * 16, 0, 16);
    const u32x4 zpart = __builtin_amdgcn_raw_buffer_load_b128(rkv, (lane & 15) * KVP_BYTES + 4096 + w * 64 + (lane >> 4) * 16, 0, 16);

    apply_mean<1>(am, negm, wn, r);
    {
      f32x16 h;
#pragma unroll
      for (int e = 0; e < 8; ++e) h[e] = am[0][e] * am[0][8 + e];
      store_frag(p.ws, tb, 96 + 2 * ct + wn, lane, pack8(h, 0, r2));
    }
    {
      float v[8] = {};
#pragma unroll
      for (int i = 0; i < 8; ++i)
#pragma unroll
        for (int e = 0; e < 4; ++e) { v[2 * e] += bf_lo(part[i][e]); v[2 * e + 1] += bf_hi(part[i][e]); }
      float t[4];
#pragma unroll
      for (int e = 0; e < 4; ++e) {
        const float give = (lane & 1) ? v[e] : v[4 + e], keep = (lane & 1) ? v[4 + e] : v[e];
        t[e] = keep + dpp<0xB1>(give);
      }
      const int dl = lane & 15, d = 8 * ((dl & 7) >> 2) + 4 * (dl >> 3) + (dl & 3);
      *(LAS u32x2*)(L_KVT + w * 544 + d * 32 + (lane >> 4) * 8) = u32x2{pack2(t[0], t[1]), pack2(t[2], t[3])};
      f32x4 z = __builtin_bit_cast(f32x4, zpart);
#pragma unroll
      for (int e = 0; e < 4; ++e) {
        float s = z[e];
        s += dpp<0xB1>(s); s += dpp<0x4E>(s); s += dpp<0x141>(s); s += dpp<0x140>(s);
        z[e] = s;
      }
      if (dl == 0) *(LAS u32x2*)(L_KVT + w * 544 + 512 + (lane >> 4) * 8) = u32x2{pack2(z[0], z[1]), pack2(z[2], z[3])};
    }
    wg_barrier();
    STAMP(0, 8);
#pragma unroll
    for (int s = 0; s < 2; ++s) {
      const unsigned a = L_KVT + (2 * wn + s) * 544 + (r < 16 ? r : 16) * 32 + 16 * hh;
      f32x16 o = mfma32(lds_r128(a), fq[s][0], zero16);
      o = mfma32(lds_r128(a + 4 * 544), fq[s][1], o);
      store_frag(p.ws, tb, 112 + 4 * ct + 2 * wn + s, lane, pack8(o, 0, __builtin_amdgcn_rcpf(o[8] + 1e-6f)));
    }
    STAMP(0, 9);
  }
}

__global__ __launch_bounds__(512, 2) void hh_proj_kernel(Args p) {
  extern __shared__ __attribute__((aligned(16))) char smem[];
  const int xcd = blockIdx.x & 7, j = blockIdx.x >> 3;
  const int rt = 4 * (xcd & 3) + (j & 3), ct = 4 * (xcd >> 2) + (j >> 3);
  if ((j >> 2) & 1) k1_body<true>(p, rt, ct);
  else k1_body<false>(p, rt, ct);
}

__global__ __launch_bounds__(512, 2) void hh_out_kernel(Args p) {
  extern __shared__ __attribute__((aligned(16))) char smem[];
  const int tid = threadIdx.x, lane = tid & 63, w = __builtin_amdgcn_readfirstlane(tid >> 6);
  const int kh = w >> 2, v = w & 3, wm = w & 1, wn = (w >> 1) & 1, r = lane & 31, hh = lane >> 5;
  const int blk = (lane >> 4) & 1, q4 = (lane & 15) >> 2, p4 = lane & 3;
  const int xcd = blockIdx.x & 7, j = blockIdx.x >> 3;
  const int rt = 8 * (xcd & 3) + (j & 7), ct = 4 * (xcd >> 2) + (j >> 3);

  STAMP(1, 0);
  const size_t xo = (size_t)(rt * 64 + wm * 32 + r) * LD_X + (ct * 64 + wn * 32 + kh * 16 + hh * 8) * 2;
  const u32x4 xin = *(const GAS u32x4*)(p.x + xo);

  const i32x4 sa = make_srd(p.ws + WS_AFRAG, 64 * NKS * 1024);
  const i32x4 sd = make_srd(p.wd, 1792 * LD_W2), so = make_srd(p.wo, 512 * LD_W2);
  const unsigned va = lane * 16;
  const unsigned vw = (16 * v + (lane >> 3)) * LD_W2 + ((lane & 7) ^ (((lane >> 4) & 1) << 2)) * 16;
  const unsigned a0 = ((2 * rt + (v >> 1)) * NKS + 72 * kh + 2 * (v & 1)) * 1024;
  const unsigned lw = kh * 16384 + v * 2048;
  auto issue = [&](int i) {
    if (i >= 18) return;
    const unsigned l = (i & 3) * 32768 + lw;
    dma16(l, va, sa, a0 + i * 4096);
    dma16(l + 1024, va, sa, a0 + i * 4096 + 1024);
    const bool from_wo = i >= 10 && kh;
    const i32x4 s = from_wo ? so : sd;
    const unsigned o = ct * 128 + (from_wo ? (i - 10) * 64 : kh * 1152 + i * 64) * LD_W2;
    dma16(l + 8192, vw, s, o);
    dma16(l + 8192 + 1024, vw, s, o + 8 * LD_W2);
  };
  const unsigned ra = kh * 16384 + wm * 4096 + lane * 16;
  const unsigned rw = kh * 16384 + 8192 + (4 * hh + q4) * 128 + ((wn ^ (q4 >> 1)) * 64) + 32 * blk + 8 * p4;

  issue(0); issue(1); issue(2);
  wait_vm<8>();
  wg_barrier();
  issue(3);
  STAMP(1, 1);
  f32x16 acc = {}, accb = {};
  u32x4 fa[2], fw[2];
  fa[0] = lds_r128(ra);
  fw[0] = lds_tr2(rw, rw + 1024);
#pragma unroll
  for (int idx = 0; idx < 72; ++idx) {
    const int i = idx / 4, s = idx & 3;
    if (s == 3 && i < 17) {
      if (i < 15) wait_vm<8>(); else if (i == 15) wait_vm<4>(); else wait_vm<0>();
      wg_barrier();
      issue(i + 4);
    }
    if (idx + 1 < 72) {
      const unsigned o = (((idx + 1) / 4) & 3) * 32768, sn = (idx + 1) & 3;
      fa[(idx + 1) & 1] = lds_r128(ra + o + sn * 1024);
      fw[(idx + 1) & 1] = lds_tr2(rw + o + sn * 2048, rw + o + sn * 2048 + 1024);
    }
    if (idx == 40 && kh) { accb = acc; acc = f32x16{}; }
    acc = mfma32(fw[idx & 1], fa[idx & 1], acc);
  }
  STAMP(1, 2);
  const unsigned ex = v * 6144 + lane * 16;
  float b[8], sv[8];
  if (kh) {
    lds_w128(ex, __builtin_bit_cast(u32x4, f32x4{accb[0], accb[1], accb[2], accb[3]}));
    lds_w128(ex + 1024, __builtin_bit_cast(u32x4, f32x4{accb[4], accb[5], accb[6], accb[7]}));
    lds_w128(ex + 2048, __builtin_bit_cast(u32x4, f32x4{acc[0], acc[1], acc[2], acc[3]}));
    lds_w128(ex + 3072, __builtin_bit_cast(u32x4, f32x4{acc[4], acc[5], acc[6], acc[7]}));
  } else {
    lds_w128(ex + 4096, __builtin_bit_cast(u32x4, f32x4{acc[8], acc[9], acc[10], acc[11]}));
    lds_w128(ex + 5120, __builtin_bit_cast(u32x4, f32x4{acc[12], acc[13], acc[14], acc[15]}));
  }
  wg_barrier();
  if (kh) {
    const f32x4 t0 = __builtin_bit_cast(f32x4, lds_r128(ex + 4096)), t1 = __builtin_bit_cast(f32x4, lds_r128(ex + 5120));
#pragma unroll
    for (int e = 0; e < 4; ++e) {
      b[e] = accb[8 + e] + t0[e]; b[4 + e] = accb[12 + e] + t1[e];
      sv[e] = acc[8 + e]; sv[4 + e] = acc[12 + e];
    }
  } else {
    const f32x4 t0 = __builtin_bit_cast(f32x4, lds_r128(ex)), t1 = __builtin_bit_cast(f32x4, lds_r128(ex + 1024));
    const f32x4 t2 = __builtin_bit_cast(f32x4, lds_r128(ex + 2048)), t3 = __builtin_bit_cast(f32x4, lds_r128(ex + 3072));
#pragma unroll
    for (int e = 0; e < 4; ++e) {
      b[e] = acc[e] + t0[e]; b[4 + e] = acc[4 + e] + t1[e];
      sv[e] = t2[e]; sv[4 + e] = t3[e];
    }
  }
  float y[8];
#pragma unroll
  for (int e = 0; e < 8; ++e) {
    const float sg = __builtin_amdgcn_rcpf(1.f + __builtin_amdgcn_exp2f(b[e] * -1.44269504088896f));
    y[e] = sv[e] * (b[e] * sg);
  }
#pragma unroll
  for (int e = 0; e < 4; ++e) {
    const auto t = __builtin_amdgcn_permlane32_swap(asu(y[e]), asu(y[4 + e]), false, false);
    y[e] = asf(t[0]);
    y[4 + e] = asf(t[1]);
  }
  u32x4 o;
#pragma unroll
  for (int e = 0; e < 4; ++e) o[e] = pack2(bf_lo(xin[e]) + y[2 * e], bf_hi(xin[e]) + y[2 * e + 1]);
  *(GAS u32x4*)(p.out + xo) = o;
  STAMP(1, 3);
}

__global__ __launch_bounds__(512) void pre_kernel(Args a, int mode) {
  const int t = blockIdx.x * 512 + threadIdx.x;
  const char* p[8] = {a.x, a.wqkv, a.wo, a.wg, a.wu, a.wd, a.ws, a.out};
  const int n16[8] = {131072, 98304, 32768, 114688, 114688, 114688, 64 * 144 * 64 + 40000, 131072};
  u32x4 s = {};
  if (mode & 1) {
#pragma unroll
    for (int i = 0; i < 6; ++i) if (t < n16[i] && ((mode & 8) == 0 || i == 2 || i == 5) && ((mode & 16) == 0 || (i != 2 && i != 5))) s ^= ((const u32x4*)p[i])[t];
  }
  if (mode & 2) {
#pragma unroll
    for (int i = 0; i < 8; ++i) if (threadIdx.x * 4096 < n16[i]) s[0] ^= *(const unsigned*)(p[i] + threadIdx.x * 65536);
  }
  if (mode & 4) {
#pragma unroll
    for (int i = 0; i < 5; ++i) if (t + i * 131072 < 64 * 144 * 64) s ^= ((const u32x4*)a.ws)[t + i * 131072];
    s ^= ((const u32x4*)a.out)[t];
  }
  asm volatile("" :: "v"(s));
}
extern "C" void kernel_launch(void* const* d_in, const int* in_sizes, int n_in, void* d_out, int out_size,
                              void* d_ws, size_t ws_size, hipStream_t stream) {
  Args a;
  a.x = (const char*)d_in[0]; a.wqkv = (const char*)d_in[1]; a.wphi = (const char*)d_in[2]; a.wo = (const char*)d_in[3];
  a.wg = (const char*)d_in[5]; a.wu = (const char*)d_in[6]; a.wd = (const char*)d_in[7];
  a.ws = (char*)d_ws; a.out = (char*)d_out;
  (void)hipFuncSetAttribute((const void*)hh_proj_kernel, hipFuncAttributeMaxDynamicSharedMemorySize, LDS1);
  (void)hipFuncSetAttribute((const void*)hh_out_kernel, hipFuncAttributeMaxDynamicSharedMemorySize, LDS2);
  pre_kernel<<<256, 512, 0, stream>>>(a, 9);
  hh_proj_kernel<<<256, 512, LDS1, stream>>>(a);
  hh_out_kernel<<<256, 512, LDS2, stream>>>(a);
}
```

```cpp
#include <hip/hip_runtime.h>
#include <hip/hip_bf16.h>
#include <stdint.h>

typedef unsigned long long u64;
typedef __attribute__((ext_vector_type(8))) __bf16 b16x8;
typedef __attribute__((ext_vector_type(2))) __bf16 b16x2;
typedef __attribute__((ext_vector_type(4))) short s16x4;
typedef __attribute__((ext_vector_type(2))) float f32x2;
typedef __attribute__((ext_vector_type(4))) float f32x4;
typedef __attribute__((ext_vector_type(16))) float f32x16;
typedef __attribute__((ext_vector_type(2))) unsigned u32x2;
typedef __attribute__((ext_vector_type(4))) unsigned u32x4;
typedef __attribute__((ext_vector_type(4))) int i32x4;
#define LAS __attribute__((address_space(3)))
#define GAS __attribute__((address_space(1)))
#define DI __device__ __forceinline__
#define SB() __builtin_amdgcn_sched_barrier(0)

constexpr int LD_X = 1024, LD_QKV = 3072, LD_GU = 3584, LD_W2 = 1024;
constexpr int NKS = 144;
constexpr unsigned WS_AFRAG = 0;
constexpr unsigned WS_KVP = 64u * NKS * 1024;
constexpr unsigned KVP_BYTES = 4608;
constexpr unsigned WS_STAT = WS_KVP + 8 * 16 * KVP_BYTES;
constexpr unsigned WS_FKV = WS_STAT + 2048 * 16;
constexpr unsigned WS_FH = WS_FKV + 8 * 128;
constexpr unsigned WS_ARR = WS_FH + 16 * 128;
constexpr unsigned WS_EPOCH = WS_ARR + 256 * 8;
constexpr unsigned WS_BYTES = WS_EPOCH + 128;
constexpr unsigned OOB = 0x7ffffff0u;
constexpr unsigned SPIN_CAP = 1u << 20;

constexpr int L_X = 0;
constexpr int L_PK = 0;
constexpr int L_V = 8 * 4352;
constexpr int L_PHI = 53248;
constexpr int L_CS = 55296;
constexpr int L_SINK = 58368;
constexpr int L_KVT = 60416;
constexpr int L_W = 65536;
constexpr int LDS_BYTES = 163840;
constexpr int l_round(int i) { return i % 3 == 0 ? 0 : i % 3 == 1 ? 65536 : 114688; }

#define STAMP(n)

struct Args {
  const char* x; const char* wqkv; const char* wphi; const char* wo;
  const char* wg; const char* wu; const char* wd;
  char* ws; char* out;
};

DI i32x4 make_srd(const char* p, unsigned bytes) {
  const u64 a = (u64)p;
  i32x4 r;
  r[0] = __builtin_amdgcn_readfirstlane((int)(unsigned)a);
  r[1] = __builtin_amdgcn_readfirstlane((int)(unsigned)(a >> 32) & 0xffff);
  r[2] = (int)bytes;
  r[3] = 0x00020000;
  return r;
}
DI void dma16(unsigned lds, unsigned voff, const i32x4& srd, unsigned soff) {
  asm volatile("s_mov_b32 m0, %0\n\ts_nop 0\n\tbuffer_load_dwordx4 %1, %2, %3 offen lds"
               :: "s"(lds), "v"(voff), "s"(srd), "s"(soff) : "memory");
}
DI void dma4(unsigned lds, unsigned voff, const i32x4& srd, unsigned soff) {
  asm volatile("s_mov_b32 m0, %0\n\ts_nop 0\n\tbuffer_load_dword %1, %2, %3 offen lds"
               :: "s"(lds), "v"(voff), "s"(srd), "s"(soff) : "memory");
}
DI void dma16g(unsigned lds, const char* gptr) {
  asm volatile("s_mov_b32 m0, %0\n\ts_nop 0\n\tglobal_load_lds_dwordx4 %1, off" :: "s"(lds), "v"(gptr) : "memory");
}
template <int IMM> DI void load16_sc1(u32x4& d, unsigned voff, const i32x4& srd, unsigned soff) {
  asm volatile("buffer_load_dwordx4 %0, %1, %2, %3 offen offset:%4 sc1" : "=v"(d) : "v"(voff), "s"(srd), "s"(soff), "n"(IMM) : "memory");
}
DI void load16(u32x4& d, unsigned voff, const i32x4& srd, unsigned soff) {
  asm volatile("buffer_load_dwordx4 %0, %1, %2, %3 offen" : "=v"(d) : "v"(voff), "s"(srd), "s"(soff) : "memory");
}
DI void load8_sc1(u32x2& d, unsigned voff, const i32x4& srd, unsigned soff) {
  asm volatile("buffer_load_dwordx2 %0, %1, %2, %3 offen sc1" : "=v"(d) : "v"(voff), "s"(srd), "s"(soff) : "memory");
}
DI void store16_sc1(u32x4 v, unsigned voff, const i32x4& srd, unsigned soff) {
  asm volatile("buffer_store_dwordx4 %0, %1, %2, %3 offen sc1\n\ts_nop 1" :: "v"(v), "v"(voff), "s"(srd), "s"(soff) : "memory");
}
DI void store8_sc1(u32x2 v, unsigned voff, const i32x4& srd, unsigned soff) {
  asm volatile("buffer_store_dwordx2 %0, %1, %2, %3 offen sc1\n\ts_nop 1" :: "v"(v), "v"(voff), "s"(srd), "s"(soff) : "memory");
}
template <int N> DI void wait_vm() { asm volatile("s_waitcnt vmcnt(%0)" :: "n"(N) : "memory"); }
template <int N, class T> DI void wait_for(T& v) { asm volatile("s_waitcnt vmcnt(%1)" : "+v"(v) : "n"(N) : "memory"); }
template <class T> DI void pin(T& v) { asm volatile("" : "+v"(v)); }
DI void wg_barrier() {
  asm volatile("s_waitcnt lgkmcnt(0)" ::: "memory");
  __builtin_amdgcn_s_barrier();
  asm volatile("" ::: "memory");
}
DI u32x4 lds_r128(unsigned a) { return *(const LAS u32x4*)a; }
DI void lds_w128(unsigned a, u32x4 v) { *(LAS u32x4*)a = v; }
DI u32x4 lds_tr2(unsigned a0, unsigned a1) {
  const s16x4 lo = __builtin_amdgcn_ds_read_tr16_b64_v4i16((LAS s16x4*)a0);
  const s16x4 hi = __builtin_amdgcn_ds_read_tr16_b64_v4i16((LAS s16x4*)a1);
  const u32x2 l = __builtin_bit_cast(u32x2, lo), h = __builtin_bit_cast(u32x2, hi);
  return u32x4{l[0], l[1], h[0], h[1]};
}
DI f32x16 mfma32(u32x4 a, u32x4 b, f32x16 c) {
  return __builtin_amdgcn_mfma_f32_32x32x16_bf16(__builtin_bit_cast(b16x8, a), __builtin_bit_cast(b16x8, b), c, 0, 0, 0);
}
DI f32x4 mfma16(u32x4 a, u32x4 b, f32x4 c) {
  return __builtin_amdgcn_mfma_f32_16x16x32_bf16(__builtin_bit_cast(b16x8, a), __builtin_bit_cast(b16x8, b), c, 0, 0, 0);
}
DI unsigned pack2(float lo, float hi) {
  const b16x2 v = __builtin_convertvector(f32x2{lo, hi}, b16x2);
  return __builtin_bit_cast(unsigned, v);
}
DI float asf(unsigned v) { return __builtin_bit_cast(float, v); }
DI unsigned asu(float v) { return __builtin_bit_cast(unsigned, v); }
DI float bf_lo(unsigned v) { return asf(v << 16); }
DI float bf_hi(unsigned v) { return asf(v & 0xffff0000u); }
DI float half_sum(float v) {
  const auto r = __builtin_amdgcn_permlane32_swap(asu(v), asu(v), false, false);
  return asf(r[0]) + asf(r[1]);
}
DI float half_max(float v) {
  const auto r = __builtin_amdgcn_permlane32_swap(asu(v), asu(v), false, false);
  return fmaxf(asf(r[0]), asf(r[1]));
}
template <int CTRL> DI float dpp(float v) {
  return asf((unsigned)__builtin_amdgcn_update_dpp(0, (int)asu(v), CTRL, 0xf, 0xf, true));
}
DI float row_sum(float s) {
  s += dpp<0xB1>(s); s += dpp<0x4E>(s); s += dpp<0x141>(s); s += dpp<0x140>(s);
  return s;
}
DI float wave_sum(float s) {
  s = row_sum(s);
  const auto r = __builtin_amdgcn_permlane16_swap(asu(s), asu(s), false, false);
  return half_sum(asf(r[0]) + asf(r[1]));
}
DI u32x4 pack8(const f32x16& a, int o, float s) {
  return u32x4{pack2(a[o] * s, a[o + 1] * s), pack2(a[o + 2] * s, a[o + 3] * s),
               pack2(a[o + 4] * s, a[o + 5] * s), pack2(a[o + 6] * s, a[o + 7] * s)};
}
constexpr unsigned ONES2 = 0x3f803f80u;
template <int V> struct IC { static constexpr int value = V; };
template <int I, int N, class F> DI void static_for(F&& f) {
  if constexpr (I < N) { f(IC<I>{}); static_for<I + 1, N>(f); }
}

DI void dual_softmax(const f32x16& p, int o, u32x4& pos, u32x4& neg) {
  const float L2E = 1.44269504088896f;
  float mx = p[o], mn = p[o];
#pragma unroll
  for (int i = 1; i < 8; ++i) { mx = fmaxf(mx, p[o + i]); mn = fminf(mn, p[o + i]); }
  mx = half_max(mx);
  mn = -half_max(-mn);
  const float mxl = mx * L2E, mnl = mn * L2E;
  float ep[8], en[8], sp = 0.f, sn = 0.f;
#pragma unroll
  for (int i = 0; i < 8; ++i) {
    ep[i] = __builtin_amdgcn_exp2f(fmaf(p[o + i], L2E, -mxl));
    en[i] = __builtin_amdgcn_exp2f(fmaf(p[o + i], -L2E, mnl));
    sp += ep[i]; sn += en[i];
  }
  const float rp = __builtin_amdgcn_rcpf(half_sum(sp)), rn = __builtin_amdgcn_rcpf(half_sum(sn));
#pragma unroll
  for (int i = 0; i < 4; ++i) {
    pos[i] = pack2(ep[2 * i] * rp, ep[2 * i + 1] * rp);
    neg[i] = pack2(en[2 * i] * rn, en[2 * i + 1] * rn);
  }
}
DI void poll16(const i32x4& sws, unsigned off, u32x2 epoch, int lane) {
  for (unsigned spin = 0; spin < SPIN_CAP; ++spin) {
    u32x2 f;
    load8_sc1(f, (lane & 15) * 8, sws, off);
    wait_for<0>(f);
    if (__all(f[0] == epoch[0] && f[1] == epoch[1])) break;
    __builtin_amdgcn_s_sleep(1);
  }
}

template <bool A> constexpr int n_units(int g) { return g < 8 ? 3 : g < 16 ? (A ? 1 : 3) : 0; }
constexpr int n_x(int j) { return j < 8 ? 2 : 0; }
constexpr int n_extra(int t) { return (t == 0 || t == 3 || t == 6 || (t >= 9 && t <= 14)) ? 1 : 0; }
constexpr int N_PRO_TAIL = 2;
template <bool A> constexpr int vm_at(int g) {
  if (g == 0) return 3 + 2 + 3 + N_PRO_TAIL;
  if (g == 1) return 3 + N_PRO_TAIL + n_extra(0) + n_x(2) + n_units<A>(3);
  if (g < 8) return n_units<A>(g + 1) + n_extra(g - 1) + n_x(g + 1) + n_units<A>(g + 2);
  if (g == 8) return n_units<A>(9) + n_units<A>(10);
  return n_extra(g - 2) + n_units<A>(g + 1) + n_extra(g - 1) + n_units<A>(g + 2);
}

struct Ctx {
  i32x4 sx, s0, s1, sws, sd, so;
  unsigned vx0, vx1, vw, vw2;
  const char* vmix;
  unsigned xrow;
  unsigned col;
  unsigned col2;
  int w, lane, rt, ct, rt2;
  unsigned xa[4];
  unsigned wa[2][4];
  u32x2 epoch;
  u32x4 srow, stat;
  const char* wg; const char* wu;
};

DI void issue_x(const Ctx& c, int j, int n) {
  if (j < 8) dma16(L_X + (j % 3) * 16384 + c.w * 2048 + n * 1024, n ? c.vx1 : c.vx0, c.sx, c.xrow + 128 * j);
}
template <bool A> DI void issue_w(Ctx& c, int g, int u) {
  if (u >= n_units<A>(g)) return;
  const unsigned l = L_W + (g & 3) * 24576 + u * 8192 + c.w * 1024;
  const unsigned k0 = (g & 7) * 64;
  if (A) {
    if (g < 8) {
      dma16(l, c.vw, c.s0, c.col + k0 * LD_QKV + (u == 0 ? 1024 : u == 1 ? 2048 : 0));
    } else {
      dma16g(l, c.vmix);
      c.vmix += 64 * LD_GU;
    }
  } else {
    const int n = (g < 8 ? 0 : 3) + u;
    dma16(l, c.vw, (n & 1) ? c.s1 : c.s0, c.col + k0 * LD_GU + (n >> 1) * 128);
  }
}
DI void issue_w2(const Ctx& c, int i, int n) {
  const int q = c.w & 3, m = c.w >> 2;
  const int ks = 36 * q + 6 * i + 3 * m + (n >> 1);
  const bool wo = ks >= 112;
  const i32x4 s = wo ? c.so : c.sd;
  dma16(l_round(i) + q * 12288 + (6 * m + n) * 1024, c.vw2, s, c.col2 + ((wo ? ks - 112 : ks) * 16 + (n & 1) * 8) * LD_W2);
}
template <bool A> DI void touch(const Ctx& c, int which) {
  const unsigned sink = L_SINK + c.w * 256;
  const int odd = c.w & 1, hw = c.w >> 1, l = c.lane;
  if (which == 0) {
    if (A) {
      const int i = c.w * 8 + l, row = 192 + 20 * c.rt + i / 3;
      dma4(sink, (l < 8 && i < 60) ? row * LD_QKV + (i % 3) * 1024 : OOB, c.s0, c.col);
    } else {
      const int n = odd ? 5 : 10, i = hw * n + l, row = 192 + 20 * c.rt + (odd ? i : i >> 1);
      dma4(sink, l < n ? row * LD_GU + (odd ? 0 : (i & 1) * 128) : OOB, odd ? c.s1 : c.s0, c.col);
    }
  } else if (which == 1) {
    if (A) {
      const int row = 32 * c.rt + hw * 8 + l;
      dma4(sink, l < 8 ? row * LD_GU : OOB, make_srd(odd ? c.wu : c.wg, 512 * LD_GU), 3072 + 64 * c.ct);
    } else {
      const int n = odd ? 16 : 8, i = hw * n + l, row = 32 * c.rt + (odd ? i >> 1 : i);
      dma4(sink, l < n ? row * LD_GU + (odd ? 128 + (i & 1) * 128 : 256) : OOB, odd ? c.s1 : c.s0, c.col);
    }
  } else {
    const bool wo = c.w == 7;
    const int row = wo ? 16 * c.rt2 + l : 56 * c.rt2 + 8 * c.w + l;
    dma4(sink, l < (wo ? 16 : 8) ? row * LD_W2 : OOB, wo ? c.so : c.sd, c.col2);
  }
}
template <bool A> DI void issue_extra(Ctx& c, int t) {
  if (t == 0) touch<A>(c, 1);
  if (t == 3) touch<A>(c, 2);
  if (t == 6) load16_sc1<0>(c.stat, (32 * (c.w & 3) + (c.lane & 31)) * 16, c.sws, WS_STAT + c.rt * 2048);
  if (t >= 9 && t <= 14) issue_w2(c, 0, t - 9);
}
template <bool A> DI void refill(Ctx& c, int t, int n) {
  if (n == 0) issue_extra<A>(c, t);
  if (n < 2) issue_x(c, t + 2, n);
  else issue_w<A>(c, t + 3, n - 2);
}
template <bool A, int G> DI void open_stage() { wait_vm<vm_at<A>(G)>(); wg_barrier(); }
DI u32x4 read_w(const Ctx& c, int g, int i, int u) {
  const unsigned a = c.wa[(g >> 1) & 1][i] + (g & 1) * 24576 + u * 8192;
  return lds_tr2(a, a + 512);
}

struct RowStat { float v[8]; float s; float mean; };
DI void stat_step(Ctx& c, RowStat& t, int n, int c16) {
  if (n == 0) {
    pin(c.srow); pin(c.epoch);
#pragma unroll
    for (int e = 0; e < 4; ++e) { t.v[2 * e] = bf_lo(c.srow[e]); t.v[2 * e + 1] = bf_hi(c.srow[e]); pin(t.v[2 * e]); pin(t.v[2 * e + 1]); }
  }
  if (n == 1) { t.s = ((t.v[0] + t.v[1]) + (t.v[2] + t.v[3])) + ((t.v[4] + t.v[5]) + (t.v[6] + t.v[7])); pin(t.s); }
  if (n == 2) { t.s = row_sum(t.s); pin(t.s); }
  if (n == 3) {
    const auto r = __builtin_amdgcn_permlane16_swap(asu(t.s), asu(t.s), false, false);
    t.mean = half_sum(asf(r[0]) + asf(r[1])) * (1.f / 512.f); pin(t.mean);
  }
  if (n == 4) {
#pragma unroll
    for (int e = 0; e < 8; ++e) { t.v[e] -= t.mean; pin(t.v[e]); }
  }
  if (n == 5) {
    float a = t.v[0] * t.v[0], b = t.v[1] * t.v[1];
#pragma unroll
    for (int e = 2; e < 8; e += 2) { a = fmaf(t.v[e], t.v[e], a); b = fmaf(t.v[e + 1], t.v[e + 1], b); }
    t.s = a + b; pin(t.s);
  }
  if (n == 6) { t.s = row_sum(t.s); pin(t.s); }
  if (n == 7) {
    const auto r = __builtin_amdgcn_permlane16_swap(asu(t.s), asu(t.s), false, false);
    t.s = __builtin_amdgcn_rsqf(half_sum(asf(r[0]) + asf(r[1])) * (1.f / 512.f) + 1e-5f); pin(t.s);
  }
  if (n == 8) {
    const u64 e = (((u64)c.epoch[1] << 32) | c.epoch[0]) + 1;
    c.epoch = u32x2{(unsigned)e, (unsigned)(e >> 32)};
    store16_sc1(u32x4{asu(t.mean), c.epoch[0], asu(t.s), c.epoch[0]}, c.lane == 0 ? 0 : OOB, c.sws,
                WS_STAT + (c.rt * 128 + c16 * 8 + c.w) * 16);
    if (c.w == 0) store8_sc1(c.epoch, c.lane == 0 ? 0 : OOB, c.sws, WS_ARR + blockIdx.x * 8);
  }
}

template <bool A, int NU, int G0, int PD, bool OPEN_NEXT>
DI void gemm_pass(Ctx& c, u32x4 (&xf)[32], f32x16 (&acc)[NU], float (&cs)[NU], int c16) {
  u32x4 wf[PD][NU];
  RowStat rs;
  if (G0 == 0) xf[0] = lds_r128(c.xa[0]);
#pragma unroll
  for (int u = 0; u < NU; ++u) {
#pragma unroll
    for (int d = 0; d < PD; ++d) wf[d][u] = read_w(c, G0, d, u);
    cs[u] = 0.f;
#pragma unroll
    for (int e = 0; e < 16; ++e) acc[u][e] = 0.f;
  }
  SB();
  static_for<0, 32 * NU>([&](auto ic) {
    constexpr int idx = decltype(ic)::value / NU, u = decltype(ic)::value % NU;
    constexpr int g = G0 + idx / 4, i = idx & 3, gn = G0 + (idx + PD) / 4, in = (idx + PD) & 3;
    constexpr bool more = idx + PD < 32, opens = i == 4 - PD && (g < G0 + 7 || OPEN_NEXT);
    u32x4& f = wf[idx % PD][u];
    acc[u] = mfma32(f, xf[idx], acc[u]);
    SB();
    cs[u] += bf_lo(f[i]);
    cs[u] += bf_hi(f[i]);
    pin(cs[u]);
    SB();
    if constexpr (u == 0 && opens) { open_stage<A, g + 1>(); SB(); }
    if constexpr (more) {
      if constexpr (u == 0 && G0 == 0) xf[idx + 1] = lds_r128(c.xa[in] + (gn % 3) * 16384);
      f = read_w(c, gn, in, u);
    }
    if constexpr (G0 == 0 && g == 2 && i * 3 + u < 9) stat_step(c, rs, i * 3 + u, c16);
    if constexpr (NU == 3) {
      if constexpr (opens && u > 0) refill<A>(c, g + 1, u - 1);
      if constexpr (i < 3 && u == 2) refill<A>(c, g, i + 2);
    } else {
      if constexpr (opens) refill<A>(c, g + 1, 0);
      if constexpr (i == 0) refill<A>(c, g, 2);
    }
    SB();
  });
}

template <int NU> DI void put_colsum(const float (&cs)[NU], int wm, int wn, int r) {
#pragma unroll
  for (int u = 0; u < NU; ++u) {
    const float t = half_sum(cs[u]);
    const float hi = bf_lo(pack2(t, 0.f));
    *(LAS unsigned*)(L_CS + ((u * 2 + wn) * 32 + r) * 16 + wm * 4) = pack2(hi, t - hi);
  }
}
template <int NU> DI void apply_mean(f32x16 (&acc)[NU], u32x4 negm, int wn, int r) {
#pragma unroll
  for (int u = 0; u < NU; ++u) acc[u] = mfma32(lds_r128(L_CS + ((u * 2 + wn) * 32 + r) * 16), negm, acc[u]);
}
DI void store_frag(const Ctx& c, int tb, int ks, u32x4 v) {
  store16_sc1(v, c.lane * 16, c.sws, WS_AFRAG + (tb * NKS + ks) * 1024);
}
DI void store_h(const Ctx& c, int tb, int ks, const f32x16& g, const f32x16& u, float r2) {
  f32x16 h;
#pragma unroll
  for (int e = 0; e < 16; ++e) h[e] = g[e] * u[e];
  store_frag(c, tb, ks, pack8(h, 0, r2));
  store_frag(c, tb, ks + 1, pack8(h, 8, r2));
}
DI void issue_w2_rounds12(const Ctx& c) {
#pragma unroll
  for (int n = 0; n < 12; ++n) issue_w2(c, 1 + n / 6, n % 6);
}

template <bool A>
DI void phase1(const Args& p, Ctx& c) {
  const int tid = threadIdx.x, lane = c.lane, w = c.w, rt = c.rt, ct = c.ct;
  const int wm = w & 3, wn = w >> 2, r = lane & 31, hh = lane >> 5;
  const int blk = (lane >> 4) & 1, q4 = (lane & 15) >> 2, p4 = lane & 3;
  const int tb = rt * 4 + wm, c16 = (A ? 8 : 0) + ct;
  {
    const int row = 16 * w + (lane >> 3), ch = (lane & 7) ^ ((row >> 1) & 7);
    c.vx0 = row * LD_X + ch * 16;
    c.vx1 = (row + 8) * LD_X + (ch ^ 4) * 16;
    const int wrow = 8 * w + (lane >> 3), wch = (lane & 7) ^ (((lane >> 4) & 1) << 2);
    c.vw2 = (lane >> 3) * LD_W2 + wch * 16;
    if (A) {
      c.s0 = make_srd(p.wqkv, 512 * LD_QKV); c.s1 = c.s0;
      c.vw = wrow * LD_QKV + wch * 16;
      c.col = ct * 128;
      c.vmix = (((wch >> 1) & 1) ? p.wu : p.wg) + wrow * LD_GU + (1536 + 32 * ct + 16 * (wch >> 2) + 8 * (wch & 1)) * 2;
    } else {
      c.s0 = make_srd(p.wg, 512 * LD_GU); c.s1 = make_srd(p.wu, 512 * LD_GU);
      c.vw = wrow * LD_GU + wch * 16;
      c.col = ct * 384;
      c.vmix = nullptr;
    }
  }
  load8_sc1(c.epoch, 0, c.sws, WS_EPOCH);
  if (A) dma4(L_PHI + w * 256, tid * 4, make_srd(p.wphi, 32 * 512), ct * 2048);
#pragma unroll
  for (int g = 0; g < 3; ++g) {
#pragma unroll
    for (int u = 0; u < 3; ++u) issue_w<A>(c, g, u);
    if (g < 2) { issue_x(c, g, 0); issue_x(c, g, 1); __builtin_amdgcn_s_barrier(); }
  }
  load16(c.srow, lane * 16, c.sx, c.xrow + (c16 * 8 + w) * LD_X);
  touch<A>(c, 0);
#pragma unroll
  for (int i = 0; i < 4; ++i) {
    const int s = (i + wm) & 3;
    c.xa[i] = L_X + (32 * wm + r) * 128 + (((2 * s + hh) ^ ((r >> 1) & 7)) * 16);
    c.wa[0][i] = L_W + 2048 * s + (8 * hh + q4) * 128 + ((wn ^ (q4 >> 1)) * 64) + 32 * blk + 8 * p4;
    c.wa[1][i] = c.wa[0][i] + 49152;
    pin(c.wa[0][i]); pin(c.wa[1][i]);
  }
  open_stage<A, 0>();
  refill<A>(c, 0, 0); refill<A>(c, 0, 1);
  STAMP(1);

  u32x4 xf[32];
  f32x16 acc[3];
  float cs[3];
  gemm_pass<A, 3, 0, 1, true>(c, xf, acc, cs, c16);
  STAMP(2);

  put_colsum<3>(cs, wm, wn, r);
  pin(c.stat);
  for (unsigned spin = 0; spin < SPIN_CAP && !__all(c.stat[1] == c.epoch[0] && c.stat[3] == c.epoch[0]); ++spin) {
    load16_sc1<0>(c.stat, (32 * wm + r) * 16, c.sws, WS_STAT + rt * 2048);
    wait_for<0>(c.stat);
  }
  const float rstd = asf(c.stat[2]);
  u32x4 negm;
  {
    const float nm = -asf(c.stat[0]);
    const float mhi = bf_lo(pack2(nm, 0.f));
    const float mm = hh ? (nm - mhi) : mhi;
    const unsigned m2 = pack2(mm, mm);
    negm = u32x4{m2, m2, m2, m2};
  }
  wg_barrier();
  apply_mean<3>(acc, negm, wn, r);
  const float r2 = rstd * rstd;

  if constexpr (!A) {
    const int ks0 = 12 * ct + 2 * wn;
    store_h(c, tb, ks0, acc[0], acc[1], r2);
    STAMP(3);
    const f32x16 g1 = acc[2];
    gemm_pass<A, 3, 8, 1, false>(c, xf, acc, cs, c16);
    STAMP(6);
    put_colsum<3>(cs, wm, wn, r);
    wg_barrier();
    issue_w2_rounds12(c);
    apply_mean<3>(acc, negm, wn, r);
    store_h(c, tb, ks0 + 4, g1, acc[0], r2);
    store_h(c, tb, ks0 + 8, acc[1], acc[2], r2);
  } else {
    u32x4 ph0, ph1;
    {
      const unsigned a = L_PHI + (2 * wn + blk) * 512 + (4 * hh + q4) * 32 + 8 * p4;
      const u32x4 wp = lds_tr2(a, a + 256);
      const u32x4 z = {0u, 0u, 0u, 0u};
      ph0 = blk ? z : wp;
      ph1 = blk ? wp : z;
    }
    const f32x16 zero16 = {};
    const unsigned img = hh * 2176 + (32 * wm + r) * 16;
    {
      f32x16 pk = mfma32(ph0, pack8(acc[0], 0, rstd), zero16);
      pk = mfma32(ph1, pack8(acc[0], 8, rstd), pk);
#pragma unroll
      for (int s = 0; s < 2; ++s) {
        u32x4 pos, neg;
        dual_softmax(pk, 8 * s, pos, neg);
        lds_w128(L_PK + (2 * wn + s) * 4352 + img, pos);
        lds_w128(L_PK + (4 + 2 * wn + s) * 4352 + img, neg);
        lds_w128(L_V + (2 * wn + s) * 4352 + img, pack8(acc[1], 8 * s, rstd));
      }
    }
    wg_barrier();
    STAMP(3);
    const unsigned kvp = WS_KVP + ct * 16 * KVP_BYTES;
    {
      const unsigned t = (p4 >> 1) * 2176 + (4 * (lane >> 4) + q4) * 16 + (p4 & 1) * 8;
      const u32x4 ones = {ONES2, ONES2, ONES2, ONES2};
      f32x4 kv = {}, z = {};
#pragma unroll
      for (int ks = 0; ks < 4; ++ks) {
        const unsigned ap = L_PK + w * 4352 + t + ks * 512, av = L_V + (w & 3) * 4352 + t + ks * 512;
        const u32x4 fa = lds_tr2(ap, ap + 256), fb = lds_tr2(av, av + 256);
        kv = mfma16(fa, fb, kv);
        z = mfma16(fa, ones, z);
      }
      store8_sc1(u32x2{pack2(kv[0], kv[1]), pack2(kv[2], kv[3])}, tid * 8, c.sws, kvp + rt * KVP_BYTES);
      asm volatile("s_nop 7" : "+v"(z));
      store16_sc1(__builtin_bit_cast(u32x4, z), (lane & 15) == 0 ? (lane >> 4) * 16 : OOB, c.sws,
                  kvp + rt * KVP_BYTES + 4096 + w * 64);
    }
    u32x4 fq[2][2];
    {
      f32x16 pq = mfma32(ph0, pack8(acc[2], 0, rstd), zero16);
      pq = mfma32(ph1, pack8(acc[2], 8, rstd), pq);
      dual_softmax(pq, 0, fq[0][0], fq[0][1]);
      dual_softmax(pq, 8, fq[1][0], fq[1][1]);
      pin(fq[0][0]); pin(fq[0][1]); pin(fq[1][0]); pin(fq[1][1]);
    }
    STAMP(4);
    wait_vm<0>();
    wg_barrier();
    if (w == 0) store8_sc1(c.epoch, lane == 0 ? 0 : OOB, c.sws, WS_FKV + ct * 128 + rt * 8);
    STAMP(5);

    f32x16 am[1];
    float csm[1];
    gemm_pass<A, 1, 8, 2, false>(c, xf, am, csm, c16);
    STAMP(6);
    put_colsum<1>(csm, wm, wn, r);
    if (w == 0) poll16(c.sws, WS_FKV + ct * 128, c.epoch, lane);
    wg_barrier();
    STAMP(7);
    u32x4 part[8], zpart;
#pragma unroll
    for (int i = 0; i < 8; ++i) load16_sc1<0>(part[i], (lane & 1) * KVP_BYTES + (tid >> 1) * 16, c.sws, kvp + 2 * i * KVP_BYTES);
    load16_sc1<0>(zpart, (lane & 15) * KVP_BYTES + (lane >> 4) * 16, c.sws, kvp + 4096 + w * 64);
    issue_w2_rounds12(c);

    apply_mean<1>(am, negm, wn, r);
    u32x4 hm;
    {
      f32x16 h;
#pragma unroll
      for (int e = 0; e < 8; ++e) h[e] = am[0][e] * am[0][8 + e];
      hm = pack8(h, 0, r2);
      pin(hm);
    }
    {
      wait_vm<12>();
#pragma unroll
      for (int i = 0; i < 8; ++i) pin(part[i]);
      pin(zpart);
      store_frag(c, tb, 96 + 2 * ct + wn, hm);
      float v[8] = {};
#pragma unroll
      for (int i = 0; i < 8; ++i)
#pragma unroll
        for (int e = 0; e < 4; ++e) { v[2 * e] += bf_lo(part[i][e]); v[2 * e + 1] += bf_hi(part[i][e]); }
      float t[4];
#pragma unroll
      for (int e = 0; e < 4; ++e) {
        const float give = (lane & 1) ? v[e] : v[4 + e], keep = (lane & 1) ? v[4 + e] : v[e];
        t[e] = keep + dpp<0xB1>(give);
      }
      const int dl = lane & 15, d = 8 * ((dl & 7) >> 2) + 4 * (dl >> 3) + (dl & 3);
      *(LAS u32x2*)(L_KVT + w * 544 + d * 32 + (lane >> 4) * 8) = u32x2{pack2(t[0], t[1]), pack2(t[2], t[3])};
      const float z0 = row_sum(asf(zpart[0])), z1 = row_sum(asf(zpart[1])), z2 = row_sum(asf(zpart[2])), z3 = row_sum(asf(zpart[3]));
      if (dl == 0) *(LAS u32x2*)(L_KVT + w * 544 + 512 + (lane >> 4) * 8) = u32x2{pack2(z0, z1), pack2(z2, z3)};
    }
    wg_barrier();
    STAMP(8);
#pragma unroll
    for (int s = 0; s < 2; ++s) {
      const unsigned a = L_KVT + (2 * wn + s) * 544 + (r < 16 ? r : 16) * 32 + 16 * hh;
      f32x16 o = mfma32(lds_r128(a), fq[s][0], zero16);
      o = mfma32(lds_r128(a + 4 * 544), fq[s][1], o);
      store_frag(c, tb, 112 + 4 * ct + 2 * wn + s, pack8(o, 0, __builtin_amdgcn_rcpf(o[8] + 1e-6f)));
    }
  }
  STAMP(9);
  wait_vm<0>();
  wg_barrier();
  if (w == 0) store8_sc1(c.epoch, lane == 0 ? 0 : OOB, c.sws, WS_FH + rt * 128 + c16 * 8);
  STAMP(10);
}

constexpr int PA = 12;
constexpr int vm_frag(int j) {
  int n = (j + PA <= 36 ? PA : 36 - j) - 1;
  if (j < PA) n += 1;
  for (int i = 1; i <= 3; ++i) if (6 * i <= j && j < 6 * i + PA) n += 6;
  return n;
}
constexpr int vm_round(int i) {
  int n = i < 5 ? 6 : 0;
  for (int j = 6 * i - 12; j < 6 * i - 1; ++j) if (j + PA < 36) n += 1;
  return n;
}
template <int J> DI void frag_load(const Ctx& c, u32x4 (&af)[PA], unsigned va, unsigned sa) {
  if constexpr (J < 36) load16_sc1<(J & 3) * 1024>(af[J % PA], va, c.sws, sa + (J >> 2) * 4096);
}
template <int J> struct P2 {
  static DI void run(const Ctx& c, u32x4 (&af)[PA], u32x4 (&wf)[2], f32x16 (&acc)[2], f32x16 (&accb)[2],
                     const unsigned (&rw)[3][2], unsigned va, unsigned sa, int q) {
    constexpr int i = J / 6, s = J % 6, in = (J + 1) / 6, sn = (J + 1) % 6;
    if (J == 4 && q == 3) { accb[0] = acc[0]; accb[1] = acc[1]; acc[0] = f32x16{}; acc[1] = f32x16{}; }
    wait_for<vm_frag(J)>(af[J % PA]);
    SB();
    acc[0] = mfma32(wf[0], af[J % PA], acc[0]);
    SB();
    if constexpr (s == 5 && i < 5) {
      if constexpr (i >= 2) wait_vm<vm_round(i + 1)>();
      wg_barrier();
      SB();
    }
    if constexpr (J + 1 < 36) wf[0] = lds_tr2(rw[in % 3][0] + sn * 2048, rw[in % 3][0] + sn * 2048 + 1024);
    SB();
    acc[1] = mfma32(wf[1], af[J % PA], acc[1]);
    SB();
    if constexpr (J + 1 < 36) wf[1] = lds_tr2(rw[in % 3][1] + sn * 2048, rw[in % 3][1] + sn * 2048 + 1024);
    frag_load<J + PA>(c, af, va, sa);
    if constexpr (s == 5 && i < 3) {
#pragma unroll
      for (int n = 0; n < 6; ++n) issue_w2(c, i + 3, n);
    }
    SB();
    if constexpr (J + 1 < 36) P2<J + 1>::run(c, af, wf, acc, accb, rw, va, sa, q);
  }
};
template <int J> DI void frag_prologue(const Ctx& c, u32x4 (&af)[PA], unsigned va, unsigned sa) {
  frag_load<J>(c, af, va, sa);
  if constexpr (J + 1 < PA) frag_prologue<J + 1>(c, af, va, sa);
}

DI void phase2(const Args& p, Ctx& c) {
  const int lane = c.lane, w = c.w, q = w & 3, m = w >> 2, r = lane & 31, hh = lane >> 5;
  const int blk = (lane >> 4) & 1, q4 = (lane & 15) >> 2, p4 = lane & 3;
  if (w == 0) poll16(c.sws, WS_FH + (c.rt2 >> 1) * 128, c.epoch, lane);
  if (w == 1 && blockIdx.x == 0) {
    for (unsigned spin = 0; spin < SPIN_CAP; ++spin) {
      u32x4 a, b;
      load16_sc1<0>(a, lane * 32, c.sws, WS_ARR);
      load16_sc1<16>(b, lane * 32, c.sws, WS_ARR);
      wait_for<0>(a); pin(b);
      const bool ok = a[0] == c.epoch[0] && a[1] == c.epoch[1] && a[2] == c.epoch[0] && a[3] == c.epoch[1] &&
                      b[0] == c.epoch[0] && b[1] == c.epoch[1] && b[2] == c.epoch[0] && b[3] == c.epoch[1];
      if (__all(ok)) break;
      __builtin_amdgcn_s_sleep(1);
    }
    store8_sc1(c.epoch, lane == 0 ? 0 : OOB, c.sws, WS_EPOCH);
  }
  wg_barrier();
  STAMP(11);
  const unsigned va = lane * 16, sa = WS_AFRAG + ((2 * c.rt2 + m) * NKS + 36 * q) * 1024;
  u32x4 af[PA], xin;
  frag_prologue<0>(c, af, va, sa);
  const unsigned xo = (c.rt2 * 64 + m * 32 + r) * LD_X + (q * 16 + hh * 8) * 2;
  load16(xin, xo, c.sx, c.col2);
  unsigned rw[3][2];
#pragma unroll
  for (int i = 0; i < 3; ++i)
#pragma unroll
    for (int t = 0; t < 2; ++t) {
      rw[i][t] = l_round(i) + q * 12288 + (4 * hh + q4) * 128 + ((t ^ (q4 >> 1)) * 64) + 32 * blk + 8 * p4;
      pin(rw[i][t]);
    }
  f32x16 acc[2] = {}, accb[2] = {};
  u32x4 wf[2];
  wf[0] = lds_tr2(rw[0][0], rw[0][0] + 1024);
  wf[1] = lds_tr2(rw[0][1], rw[0][1] + 1024);
  SB();
  P2<0>::run(c, af, wf, acc, accb, rw, va, sa, q);
  pin(xin);
  STAMP(12);
  const unsigned ex = m * 32768 + lane * 16;
  auto put = [&](int dst, int slot, const f32x16& a, int o) {
    lds_w128(ex + dst * 8192 + slot * 2048, __builtin_bit_cast(u32x4, f32x4{a[o], a[o + 1], a[o + 2], a[o + 3]}));
    lds_w128(ex + dst * 8192 + slot * 2048 + 1024, __builtin_bit_cast(u32x4, f32x4{a[o + 4], a[o + 5], a[o + 6], a[o + 7]}));
  };
  float b[8], sv[8];
  auto keep = [&](float (&d)[8], const f32x16& a, int o) {
#pragma unroll
    for (int e = 0; e < 8; ++e) d[e] = a[o + e];
  };
  if (q == 0) { keep(b, acc[0], 0); put(1, 0, acc[0], 8); put(2, 0, acc[1], 0); put(3, 0, acc[1], 8); }
  else if (q == 1) { keep(b, acc[0], 8); put(0, 0, acc[0], 0); put(2, 1, acc[1], 0); put(3, 1, acc[1], 8); }
  else if (q == 2) { keep(b, acc[1], 0); put(0, 1, acc[0], 0); put(1, 1, acc[0], 8); put(3, 2, acc[1], 8); }
  else {
    keep(b, accb[1], 8); put(0, 2, accb[0], 0); put(1, 2, accb[0], 8); put(2, 2, accb[1], 0);
    put(0, 3, acc[0], 0); put(1, 3, acc[0], 8); put(2, 3, acc[1], 0); put(3, 3, acc[1], 8);
  }
  wg_barrier();
#pragma unroll
  for (int s = 0; s < 4; ++s) {
    const f32x4 t0 = __builtin_bit_cast(f32x4, lds_r128(ex + q * 8192 + s * 2048));
    const f32x4 t1 = __builtin_bit_cast(f32x4, lds_r128(ex + q * 8192 + s * 2048 + 1024));
#pragma unroll
    for (int e = 0; e < 4; ++e) {
      if (s < 3) { b[e] += t0[e]; b[4 + e] += t1[e]; }
      else { sv[e] = t0[e]; sv[4 + e] = t1[e]; }
    }
  }
  float y[8];
#pragma unroll
  for (int e = 0; e < 8; ++e) {
    const float sg = __builtin_amdgcn_rcpf(1.f + __builtin_amdgcn_exp2f(b[e] * -1.44269504088896f));
    y[e] = sv[e] * (b[e] * sg);
  }
#pragma unroll
  for (int e = 0; e < 4; ++e) {
    const auto t = __builtin_amdgcn_permlane32_swap(asu(y[e]), asu(y[4 + e]), false, false);
    y[e] = asf(t[0]);
    y[4 + e] = asf(t[1]);
  }
  u32x4 o;
#pragma unroll
  for (int e = 0; e < 4; ++e) o[e] = pack2(bf_lo(xin[e]) + y[2 * e], bf_hi(xin[e]) + y[2 * e + 1]);
  *(GAS u32x4*)(p.out + xo + c.col2) = o;
  STAMP(13);
}

__global__ __launch_bounds__(512, 2) void hh_fused_kernel(Args p) {
  extern __shared__ __attribute__((aligned(16))) char smem[];
  STAMP(0);
  Ctx c;
  c.lane = threadIdx.x & 63;
  c.w = __builtin_amdgcn_readfirstlane(threadIdx.x >> 6);
  const int xcd = blockIdx.x & 7, j = blockIdx.x >> 3;
  c.rt = 8 * (xcd & 1) + (j & 7);
  c.ct = 2 * (xcd >> 1) + (j >> 4);
  c.rt2 = 4 * xcd + (j & 3);
  c.col2 = (j >> 2) * 128;
  c.xrow = c.rt * 128 * LD_X;
  c.sx = make_srd(p.x, 2048 * LD_X);
  c.sws = make_srd(p.ws, WS_BYTES);
  c.sd = make_srd(p.wd, 1792 * LD_W2);
  c.so = make_srd(p.wo, 512 * LD_W2);
  c.wg = p.wg; c.wu = p.wu;
  if ((j >> 3) & 1) phase1<true>(p, c);
  else phase1<false>(p, c);
  phase2(p, c);
}

extern "C" void kernel_launch(void* const* d_in, const int* in_sizes, int n_in, void* d_out, int out_size,
                              void* d_ws, size_t ws_size, hipStream_t stream) {
  Args a;
  a.x = (const char*)d_in[0]; a.wqkv = (const char*)d_in[1]; a.wphi = (const char*)d_in[2]; a.wo = (const char*)d_in[3];
  a.wg = (const char*)d_in[5]; a.wu = (const char*)d_in[6]; a.wd = (const char*)d_in[7];
  a.ws = (char*)d_ws; a.out = (char*)d_out;
  (void)hipFuncSetAttribute((const void*)hh_fused_kernel, hipFuncAttributeMaxDynamicSharedMemorySize, LDS_BYTES);
  hh_fused_kernel<<<256, 512, LDS_BYTES, stream>>>(a);
}
```

```cpp
#include <hip/hip_runtime.h>
#include <hip/hip_bf16.h>
#include <stdint.h>

typedef unsigned long long u64;
typedef __attribute__((ext_vector_type(8))) __bf16 b16x8;
typedef __attribute__((ext_vector_type(2))) __bf16 b16x2;
typedef __attribute__((ext_vector_type(4))) short s16x4;
typedef __attribute__((ext_vector_type(2))) float f32x2;
typedef __attribute__((ext_vector_type(4))) float f32x4;
typedef __attribute__((ext_vector_type(16))) float f32x16;
typedef __attribute__((ext_vector_type(2))) unsigned u32x2;
typedef __attribute__((ext_vector_type(4))) unsigned u32x4;
typedef __attribute__((ext_vector_type(4))) int i32x4;
#define LAS __attribute__((address_space(3)))
#define GAS __attribute__((address_space(1)))
#define DI __device__ __forceinline__
#define SB() __builtin_amdgcn_sched_barrier(0)

constexpr int LD_X = 1024, LD_QKV = 3072, LD_GU = 3584, LD_W2 = 1024;
constexpr int NKS = 144;
constexpr unsigned WS_AFRAG = 0;
constexpr unsigned WS_KVP = 64u * NKS * 1024;
constexpr unsigned KVP_BYTES = 4608;
constexpr unsigned WS_STAT = WS_KVP + 8 * 16 * KVP_BYTES;
constexpr unsigned WS_FKV = WS_STAT + 2048 * 16;
constexpr unsigned WS_FE = WS_FKV + 8 * 128;
constexpr unsigned WS_FH = WS_FE + 16 * 128;
constexpr unsigned WS_ARR = WS_FH + 16 * 128;
constexpr unsigned WS_EPOCH = WS_ARR + 256 * 8;
constexpr unsigned WS_BYTES = WS_EPOCH + 128;
constexpr unsigned OOB = 0x7ffffff0u;
constexpr unsigned SPIN_CAP = 1u << 20;

constexpr int L_X = 0;
constexpr int L_PK = 0;
constexpr int L_V = 8 * 4352;
constexpr int L_PHI = 53248;
constexpr int L_CS = 55296;
constexpr int L_SINK = 59392;
constexpr int L_KVT = 60416;
constexpr int L_W = 65536;
constexpr int LDS_BYTES = 163840;
template <bool K> constexpr int l_stage(int g) {
  if (K) return g < 8 ? (g % 3) * 16384 : (g - 8) % 3 == 2 ? 24576 : 49152 + ((g - 8) % 3) * 24576;
  return g < 8 ? ((g + 2) % 3) * 32768 : 32768 + (g - 8) * 8192;
}
constexpr int l_step(int j) { return (j % 18 < 6 ? j % 18 : j % 18 + 2) * 8192; }

#define STAMP(n)
#define STAMP_DUMP()

#define CODE_PF 1
#define CODE_BYTES 46000
#define ROT1 0
#define ROT2 1
struct Args {
  const char* x; const char* wqkv; const char* wphi; const char* wo;
  const char* wg; const char* wu; const char* wd;
  char* ws; char* out;
};

DI i32x4 make_srd(const char* p, unsigned bytes) {
  const u64 a = (u64)p;
  i32x4 r;
  r[0] = __builtin_amdgcn_readfirstlane((int)(unsigned)a);
  r[1] = __builtin_amdgcn_readfirstlane((int)(unsigned)(a >> 32) & 0xffff);
  r[2] = (int)bytes;
  r[3] = 0x00020000;
  return r;
}
DI i32x4 sel_srd(bool first, i32x4 a, i32x4 b) {
  i32x4 r;
  const int a0 = a[0], a1 = a[1], a2 = a[2], b0 = b[0], b1 = b[1], b2 = b[2];
  r[0] = first ? a0 : b0; r[1] = first ? a1 : b1; r[2] = first ? a2 : b2; r[3] = 0x00020000;
  return r;
}
DI void dma16(unsigned lds, unsigned voff, i32x4 srd, unsigned soff) {
  asm volatile("s_mov_b32 m0, %0\n\ts_nop 0\n\tbuffer_load_dwordx4 %1, %2, %3 offen lds"
               :: "s"(lds), "v"(voff), "s"(srd), "s"(soff) : "memory");
}
DI void dma4(unsigned lds, unsigned voff, i32x4 srd, unsigned soff) {
  asm volatile("s_mov_b32 m0, %0\n\ts_nop 0\n\tbuffer_load_dword %1, %2, %3 offen lds"
               :: "s"(lds), "v"(voff), "s"(srd), "s"(soff) : "memory");
}
DI void dma16g(unsigned lds, const char* gptr) {
  asm volatile("s_mov_b32 m0, %0\n\ts_nop 0\n\tglobal_load_lds_dwordx4 %1, off" :: "s"(lds), "v"(gptr) : "memory");
}
template <int IMM> DI void load16_sc1(u32x4& d, unsigned voff, i32x4 srd, unsigned soff) {
  asm volatile("buffer_load_dwordx4 %0, %1, %2, %3 offen offset:%4 sc1" : "=v"(d) : "v"(voff), "s"(srd), "s"(soff), "n"(IMM) : "memory");
}
DI void load16(u32x4& d, unsigned voff, i32x4 srd, unsigned soff) {
  asm volatile("buffer_load_dwordx4 %0, %1, %2, %3 offen" : "=v"(d) : "v"(voff), "s"(srd), "s"(soff) : "memory");
}
DI void load8_sc1(u32x2& d, unsigned voff, i32x4 srd, unsigned soff) {
  asm volatile("buffer_load_dwordx2 %0, %1, %2, %3 offen sc1" : "=v"(d) : "v"(voff), "s"(srd), "s"(soff) : "memory");
}
DI void store16_sc1(u32x4 v, unsigned voff, i32x4 srd, unsigned soff) {
  asm volatile("buffer_store_dwordx4 %0, %1, %2, %3 offen sc1\n\ts_nop 1" :: "v"(v), "v"(voff), "s"(srd), "s"(soff) : "memory");
}
DI void store8_sc1(u32x2 v, unsigned voff, i32x4 srd, unsigned soff) {
  asm volatile("buffer_store_dwordx2 %0, %1, %2, %3 offen sc1\n\ts_nop 1" :: "v"(v), "v"(voff), "s"(srd), "s"(soff) : "memory");
}
template <int N> DI void wait_vm() { asm volatile("s_waitcnt vmcnt(%0)" :: "n"(N) : "memory"); }
template <int N, class T> DI void wait_for(T& v) { asm volatile("s_waitcnt vmcnt(%1)" : "+v"(v) : "n"(N) : "memory"); }
template <class T> DI void pin(T& v) { asm volatile("" : "+v"(v)); }
DI void wg_barrier() {
  asm volatile("s_waitcnt lgkmcnt(0)" ::: "memory");
  __builtin_amdgcn_s_barrier();
  asm volatile("" ::: "memory");
}
DI u32x4 lds_r128(unsigned a) { return *(const LAS u32x4*)a; }
DI void lds_w128(unsigned a, u32x4 v) { *(LAS u32x4*)a = v; }
DI u32x4 lds_tr2(unsigned a0, unsigned a1) {
  const s16x4 lo = __builtin_amdgcn_ds_read_tr16_b64_v4i16((LAS s16x4*)a0);
  const s16x4 hi = __builtin_amdgcn_ds_read_tr16_b64_v4i16((LAS s16x4*)a1);
  const u32x2 l = __builtin_bit_cast(u32x2, lo), h = __builtin_bit_cast(u32x2, hi);
  return u32x4{l[0], l[1], h[0], h[1]};
}
DI f32x16 mfma32(u32x4 a, u32x4 b, f32x16 c) {
  return __builtin_amdgcn_mfma_f32_32x32x16_bf16(__builtin_bit_cast(b16x8, a), __builtin_bit_cast(b16x8, b), c, 0, 0, 0);
}
DI f32x4 mfma16(u32x4 a, u32x4 b, f32x4 c) {
  return __builtin_amdgcn_mfma_f32_16x16x32_bf16(__builtin_bit_cast(b16x8, a), __builtin_bit_cast(b16x8, b), c, 0, 0, 0);
}
DI unsigned pack2(float lo, float hi) {
  const b16x2 v = __builtin_convertvector(f32x2{lo, hi}, b16x2);
  return __builtin_bit_cast(unsigned, v);
}
DI float asf(unsigned v) { return __builtin_bit_cast(float, v); }
DI unsigned asu(float v) { return __builtin_bit_cast(unsigned, v); }
DI float bf_lo(unsigned v) { return asf(v << 16); }
DI float bf_hi(unsigned v) { return asf(v & 0xffff0000u); }
DI float half_sum(float v) {
  const auto r = __builtin_amdgcn_permlane32_swap(asu(v), asu(v), false, false);
  return asf(r[0]) + asf(r[1]);
}
DI float half_max(float v) {
  const auto r = __builtin_amdgcn_permlane32_swap(asu(v), asu(v), false, false);
  return fmaxf(asf(r[0]), asf(r[1]));
}
template <int CTRL> DI float dpp(float v) {
  return asf((unsigned)__builtin_amdgcn_update_dpp(0, (int)asu(v), CTRL, 0xf, 0xf, true));
}
DI float row_sum(float s) {
  s += dpp<0xB1>(s); s += dpp<0x4E>(s); s += dpp<0x141>(s); s += dpp<0x140>(s);
  return s;
}
DI float wave_sum(float s) {
  s = row_sum(s);
  const auto r = __builtin_amdgcn_permlane16_swap(asu(s), asu(s), false, false);
  return half_sum(asf(r[0]) + asf(r[1]));
}
DI u32x4 pack8(const f32x16& a, int o, float s) {
  return u32x4{pack2(a[o] * s, a[o + 1] * s), pack2(a[o + 2] * s, a[o + 3] * s),
               pack2(a[o + 4] * s, a[o + 5] * s), pack2(a[o + 6] * s, a[o + 7] * s)};
}
constexpr unsigned ONES2 = 0x3f803f80u;
template <int V> struct IC { static constexpr int value = V; };
template <int I, int N, class F> DI void static_for(F&& f) {
  if constexpr (I < N) { f(IC<I>{}); static_for<I + 1, N>(f); }
}

DI float max3(float a, float b, float c) { float r; asm("v_max3_f32 %0, %1, %2, %3" : "=v"(r) : "v"(a), "v"(b), "v"(c)); return r; }
DI float nmax3(float a, float b, float c) { float r; asm("v_max3_f32 %0, %1, -%2, -%3" : "=v"(r) : "v"(a), "v"(b), "v"(c)); return r; }
DI float max2(float a, float b) { float r; asm("v_max_f32 %0, %1, %2" : "=v"(r) : "v"(a), "v"(b)); return r; }
template <class F> DI float fold2(float a, float b, F op) {
  const auto r = __builtin_amdgcn_permlane32_swap(asu(a), asu(b), false, false);
  return op(asf(r[0]), asf(r[1]));
}
DI void spread2(float v, float& a, float& b) {
  const auto r = __builtin_amdgcn_permlane32_swap(asu(v), asu(v), false, false);
  a = asf(r[0]); b = asf(r[1]);
}
DI void dual_exp(const f32x16& p, int o, f32x2 (&ep)[4], f32x2 (&en)[4], float& sp, float& sn) {
  const float L2E = 1.44269504088896f;
  const float mx = max2(max3(max3(max3(p[o], p[o + 1], p[o + 2]), p[o + 3], p[o + 4]), p[o + 5], p[o + 6]), p[o + 7]);
  const float nm = nmax3(nmax3(nmax3(nmax3(-3.0e38f, p[o], p[o + 1]), p[o + 2], p[o + 3]), p[o + 4], p[o + 5]), p[o + 6], p[o + 7]);
  float a, b;
  spread2(fold2(mx, nm, max2) * -L2E, a, b);
  const f32x2 l2 = {L2E, L2E}, a2 = {a, a}, b2 = {b, b};
#pragma unroll
  for (int i = 0; i < 4; ++i) {
    const f32x2 v = {p[o + 2 * i], p[o + 2 * i + 1]};
    const f32x2 tp = __builtin_elementwise_fma(v, l2, a2), tn = __builtin_elementwise_fma(-v, l2, b2);
    ep[i] = f32x2{__builtin_amdgcn_exp2f(tp[0]), __builtin_amdgcn_exp2f(tp[1])};
    en[i] = f32x2{__builtin_amdgcn_exp2f(tn[0]), __builtin_amdgcn_exp2f(tn[1])};
  }
  const f32x2 s2 = (ep[0] + ep[1]) + (ep[2] + ep[3]), t2 = (en[0] + en[1]) + (en[2] + en[3]);
  sp = s2[0] + s2[1]; sn = t2[0] + t2[1];
}
DI u32x4 pack8s(const f32x2 (&e)[4], float s) {
  u32x4 r;
#pragma unroll
  for (int i = 0; i < 4; ++i) { const f32x2 t = e[i] * s; r[i] = pack2(t[0], t[1]); }
  return r;
}
DI void phi_k(const f32x16& p, int o, u32x4& pos, u32x4& neg) {
  f32x2 ep[4], en[4];
  float sp, sn, rp, rn;
  dual_exp(p, o, ep, en, sp, sn);
  spread2(__builtin_amdgcn_rcpf(fold2(sp, sn, [](float x, float y) { return x + y; })), rp, rn);
  pos = pack8s(ep, rp);
  neg = pack8s(en, rn);
}
DI void phi_q(const f32x16& p, int o, u32x4& pos, u32x4& neg, float& eps) {
  f32x2 ep[4], en[4];
  float sp, sn;
  dual_exp(p, o, ep, en, sp, sn);
  spread2(fold2(sp, sn, [](float x, float y) { return x + y; }), sp, sn);
#pragma unroll
  for (int i = 0; i < 4; ++i) pos[i] = pack2(ep[i][0], ep[i][1]);
  neg = pack8s(en, sp * __builtin_amdgcn_rcpf(sn));
  eps = sp * 1e-6f;
}

enum Op { OP_NONE, OP_X0, OP_X1, OP_W0, OP_W1, OP_W2, OP_W3, OP_TOUCH0, OP_TOUCH1, OP_TOUCH2, OP_STAT, OP_P2W, OP_FE };
struct Slot { int op, arg; };
constexpr int NEVER = 99, N_SLOT = 7;
template <bool K> constexpr Slot slot_at(int t, int n) {
  constexpr int NU = K ? 2 : 4;
  if (t < 8) {
    if (n == 0) return t == 0 ? Slot{OP_TOUCH0, 0} : t == 2 ? Slot{OP_TOUCH1, 0} : t == 4 ? Slot{OP_TOUCH2, 0} : t == 6 ? Slot{OP_STAT, 0} : Slot{OP_NONE, 0};
    if (t < 6) return n < 3 ? Slot{OP_X0 + n - 1, t + 2} : n - 3 < NU ? Slot{OP_W0 + n - 3, t + 2} : Slot{OP_NONE, 0};
    if (K) return n < 4 ? Slot{OP_W0 + n - 1, t + 2} : Slot{OP_NONE, 0};
    return n < 5 ? Slot{OP_W0, 8 + 4 * (t - 6) + n - 1} : Slot{OP_NONE, 0};
  }
  if (!K || t == 8) return Slot{OP_NONE, 0};
  if (t <= 13) return n < 3 ? Slot{OP_W0 + n, t + 2} : n == 3 ? Slot{OP_P2W, t - 9} : (n == 4 && t == 13) ? Slot{OP_P2W, 5} : Slot{OP_NONE, 0};
  if (n < 3) return Slot{OP_P2W, 9 + 3 * (t - 14) + n};
  return (n == 3 && t == 15) ? Slot{OP_FE, 0} : Slot{OP_NONE, 0};
}
template <bool K> constexpr int op_needed_by(Slot s) {
  if (s.op >= OP_X0 && s.op <= OP_W3) return (!K && s.arg > 8) ? 8 : s.arg;
  return s.op == OP_STAT ? 8 : NEVER;
}
template <bool K> constexpr int vm_at(int g) {
  if (g == 0) return K ? 4 : 6;
  if (g == 9) return 8;
  int n = 0;
  for (int t = g - 1; t >= 0; --t) {
    if (t == 8 && g >= 10) return n;
    for (int i = N_SLOT - 1; i >= 0; --i) {
      const Slot s = slot_at<K>(t, i);
      if (s.op != OP_NONE && op_needed_by<K>(s) <= g) return n;
      n += s.op != OP_NONE;
    }
  }
  return n;
}

struct Ctx {
  i32x4 sx, sq, sg, su, sws, sd, so;
  unsigned vx0, vx1, vwq, vwg, vw2;
  const char* vmix;
  unsigned xrow;
  unsigned col2;
  int w, lane, rt, ct, rt2;
  unsigned fe_off, fh_off;
  int rot1, rot2;
  unsigned xa[4];
  unsigned wa[2][4];
  u32x2 epoch, flag;
  u32x4 srow, stat;
};

DI int k_slice(const Ctx& c, int g) { return (g + c.rot1) & 7; }
DI void issue_x(const Ctx& c, int j, int n) {
  dma16(L_X + (j % 3) * 16384 + c.w * 2048 + n * 1024, n ? c.vx1 + 0u : c.vx0 + 0u, c.sx, c.xrow + 128 * k_slice(c, j));
}
template <bool K> DI void issue_w(const Ctx& c, int g, int u) {
  const unsigned l = L_W + l_stage<K>(g) + u * 8192 + c.w * 1024;
  const unsigned k0 = k_slice(c, g) * 64;
  if (K ? g < 8 : g >= 8) dma16(l, c.vwq, c.sq, c.ct * 128 + k0 * LD_QKV + (K ? 1024 + u * 1024 : 0));
  else if (K && u == 2) dma16g(l, c.vmix + k0 * LD_GU);
  else dma16(l, c.vwg, sel_srd(u & 1, c.su, c.sg), k0 * LD_GU + (K ? 2048 + c.ct * 128 : c.ct * 256 + (u >> 1) * 128));
}
DI int p2_ks(const Ctx& c, int j) {
  const int q = c.w & 3;
  if (j < 16) return 16 * q + ((j + 2 * c.rot2) & 15);
  if (j >= 28) return 112 + 8 * q + ((j - 28 + c.rot2) & 7);
  const int t = j - 16 + ((3 * c.rot2) >> 1);
  return 64 + 12 * q + (t >= 12 ? t - 12 : t);
}
DI void issue_w2(const Ctx& c, int j) {
  const int q = c.w & 3, m = c.w >> 2;
  dma16(l_step(j) + q * 2048 + m * 1024, c.vw2, sel_srd(j >= 28, c.so, c.sd),
        c.col2 + ((p2_ks(c, j) - (j >= 28 ? 112 : 0)) * 16 + m * 8) * LD_W2);
}
template <bool K> DI void touch(const Ctx& c, int which) {
  const unsigned sink = L_SINK + (c.w & 3) * 256;
  const int odd = c.w & 1, hw = c.w >> 1, l = c.lane;
  if (which == 2) {
    const bool wo = c.w == 7;
    const int row = wo ? 16 * c.rt2 + l : 56 * c.rt2 + 8 * c.w + l;
    dma4(sink, l < (wo ? 16 : 8) ? row * LD_W2 : OOB, sel_srd(wo, c.so, c.sd), c.col2);
  } else if (K == (which == 0)) {
    const int row = 32 * c.rt + hw * 8 + l;
    dma4(sink, (l < 8 && (K || !odd)) ? row * LD_QKV : OOB, c.sq, c.ct * 128 + (K ? 1024 + odd * 1024 : 0));
  } else {
    const int row = 32 * c.rt + hw * 8 + (l >> 1);
    const unsigned cb = K ? ((l & 1) ? 3072 + 64 * c.ct : 2048 + 128 * c.ct) : 256 * c.ct + (l & 1) * 128;
    dma4(sink, l < 16 ? row * LD_GU + cb : OOB, sel_srd(odd, c.su, c.sg), 0);
  }
}
DI void flag_load(Ctx& c, unsigned off, int n) { load8_sc1(c.flag, (c.lane & (n - 1)) * 8, c.sws, off); }
DI void flag_check(Ctx& c, unsigned off, int n) {
  pin(c.flag);
  for (unsigned spin = 0; spin < SPIN_CAP && !__all(c.flag[0] == c.epoch[0] && c.flag[1] == c.epoch[1]); ++spin) {
    flag_load(c, off, n);
    wait_for<0>(c.flag);
  }
}
DI void flag_store(const Ctx& c, unsigned off) {
  if (c.w == 0) store8_sc1(c.epoch, c.lane == 0 ? 0 : OOB, c.sws, off);
}
template <bool K, int T, int N> DI void run_slot(Ctx& c) {
  constexpr Slot s = slot_at<K>(T, N);
  if constexpr (s.op == OP_X0 || s.op == OP_X1) issue_x(c, s.arg, s.op - OP_X0);
  if constexpr (s.op >= OP_W0 && s.op <= OP_W3) issue_w<K>(c, s.arg, s.op - OP_W0);
  if constexpr (s.op >= OP_TOUCH0 && s.op <= OP_TOUCH2) touch<K>(c, s.op - OP_TOUCH0);
  if constexpr (s.op == OP_STAT) load16_sc1<0>(c.stat, (32 * (c.w & 3) + (c.lane & 31)) * 16, c.sws, WS_STAT + c.rt * 2048);
  if constexpr (s.op == OP_P2W) issue_w2(c, s.arg);
  if constexpr (s.op == OP_FE) flag_load(c, c.fe_off, 8);
}
template <bool K, int G> DI void open_stage(Ctx& c) {
  wait_vm<vm_at<K>(G)>(); wg_barrier();
  if (K && G == 10) flag_store(c, WS_FKV + c.ct * 128 + c.rt * 8);
}
template <bool K> DI u32x4 read_w(const Ctx& c, int g, int i, int u) {
  const int o = l_stage<K>(g) + u * 8192;
  const unsigned a = c.wa[o >= 49152][i] + o % 49152;
  return lds_tr2(a, a + 512);
}

struct RowStat { float v[8]; float s; float mean; };
DI void stat_step(Ctx& c, RowStat& t, int n, int c16) {
  if (n == 0) {
    pin(c.srow); pin(c.epoch);
#pragma unroll
    for (int e = 0; e < 4; ++e) { t.v[2 * e] = bf_lo(c.srow[e]); t.v[2 * e + 1] = bf_hi(c.srow[e]); pin(t.v[2 * e]); pin(t.v[2 * e + 1]); }
  }
  if (n == 1) { t.s = ((t.v[0] + t.v[1]) + (t.v[2] + t.v[3])) + ((t.v[4] + t.v[5]) + (t.v[6] + t.v[7])); pin(t.s); }
  if (n == 2) { t.s = row_sum(t.s); pin(t.s); }
  if (n == 3) {
    const auto r = __builtin_amdgcn_permlane16_swap(asu(t.s), asu(t.s), false, false);
    t.mean = half_sum(asf(r[0]) + asf(r[1])) * (1.f / 512.f); pin(t.mean);
  }
  if (n == 4) {
#pragma unroll
    for (int e = 0; e < 8; ++e) { t.v[e] -= t.mean; pin(t.v[e]); }
  }
  if (n == 5) {
    float a = t.v[0] * t.v[0], b = t.v[1] * t.v[1];
#pragma unroll
    for (int e = 2; e < 8; e += 2) { a = fmaf(t.v[e], t.v[e], a); b = fmaf(t.v[e + 1], t.v[e + 1], b); }
    t.s = a + b; pin(t.s);
  }
  if (n == 6) { t.s = row_sum(t.s); pin(t.s); }
  if (n == 7) {
    const auto r = __builtin_amdgcn_permlane16_swap(asu(t.s), asu(t.s), false, false);
    t.s = __builtin_amdgcn_rsqf(half_sum(asf(r[0]) + asf(r[1])) * (1.f / 512.f) + 1e-5f); pin(t.s);
  }
  if (n == 8) {
    const u64 e = (((u64)c.epoch[1] << 32) | c.epoch[0]) + 1;
    c.epoch = u32x2{(unsigned)e, (unsigned)(e >> 32)};
    store16_sc1(u32x4{asu(t.mean), c.epoch[0], asu(t.s), c.epoch[0]}, c.lane == 0 ? 0 : OOB, c.sws,
                WS_STAT + (c.rt * 128 + c16 * 8 + c.w) * 16);
    if (c.w == 0) store8_sc1(c.epoch, c.lane == 0 ? 0 : OOB, c.sws, WS_ARR + blockIdx.x * 8);
  }
}

template <bool K, int NU, int G0, int PD, class H>
DI void gemm_pass(Ctx& c, u32x4 (&xf)[32], f32x16 (&acc)[NU], float (&cs)[NU], int c16, H&& hook) {
  u32x4 wf[PD][NU];
  RowStat rs;
  if (G0 == 0) xf[0] = lds_r128(c.xa[0]);
#pragma unroll
  for (int u = 0; u < NU; ++u) {
#pragma unroll
    for (int d = 0; d < PD; ++d) wf[d][u] = read_w<K>(c, G0, d, u);
    cs[u] = 0.f;
#pragma unroll
    for (int e = 0; e < 16; ++e) acc[u][e] = 0.f;
  }
  SB();
  static_for<0, 32 * NU>([&](auto ic) {
    constexpr int idx = decltype(ic)::value / NU, u = decltype(ic)::value % NU;
    constexpr int g = G0 + idx / 4, i = idx & 3, gn = G0 + (idx + PD) / 4, in = (idx + PD) & 3;
    constexpr bool staged = K || G0 == 0, opens = i == 3 && g < (K ? 15 : 7);
    u32x4& f = wf[idx % PD][u];
    cs[u] += bf_lo(f[i]);
    cs[u] += bf_hi(f[i]);
    pin(cs[u]);
    SB();
    acc[u] = mfma32(f, xf[idx], acc[u]);
    SB();
    if constexpr (u == 0 && opens) { open_stage<K, g + 1>(c); SB(); }
    if constexpr (u == 0 && G0 == 0 && idx + 1 < 32) xf[idx + 1] = lds_r128(c.xa[(idx + 1) & 3] + (((idx + 1) / 4) % 3) * 16384);
    if constexpr (idx + PD < 32) f = read_w<K>(c, gn, in, u);
    if constexpr (G0 == 0 && u < 2 && 2 * idx + u < 9) stat_step(c, rs, 2 * idx + u, c16);
    if constexpr (staged) {
      constexpr int t = i == 3 ? g + 1 : g, pos = i == 3 ? u : NU * (i + 1) + u;
      constexpr int n = t == 0 ? pos - NU : NU == 2 ? pos - 1 : (pos & 1) ? pos / 2 : -1;
      if constexpr (t < 16 && n >= 0 && n < N_SLOT) run_slot<K, t, n>(c);
    }
    if constexpr (u == NU - 1) hook(IC<idx>{});
    SB();
  });
#pragma unroll
  for (int u = 0; u < NU; ++u) pin(acc[u]);
}

template <int NU> DI void put_colsum(const float (&cs)[NU], int wm, int wn, int r) {
#pragma unroll
  for (int u = 0; u < NU; ++u) {
    const float t = half_sum(cs[u]);
    const float hi = bf_lo(pack2(t, 0.f));
    *(LAS unsigned*)(L_CS + ((u * 2 + wn) * 32 + r) * 16 + wm * 4) = pack2(hi, t - hi);
  }
}
template <int NU> DI void apply_mean(f32x16 (&acc)[NU], u32x4 negm, int wn, int r) {
#pragma unroll
  for (int u = 0; u < NU; ++u) acc[u] = mfma32(lds_r128(L_CS + ((u * 2 + wn) * 32 + r) * 16), negm, acc[u]);
}
DI void store_frag(const Ctx& c, int tb, int ks, u32x4 v) {
  store16_sc1(v, c.lane * 16, c.sws, WS_AFRAG + (tb * NKS + ks) * 1024);
}
DI void store_h(const Ctx& c, int tb, int ks, const f32x16& g, const f32x16& u, float r2) {
  f32x16 h;
#pragma unroll
  for (int e = 0; e < 16; ++e) h[e] = g[e] * u[e];
  store_frag(c, tb, ks, pack8(h, 0, r2));
  store_frag(c, tb, ks + 1, pack8(h, 8, r2));
}

template <bool K>
DI void phase1(const Args& p, Ctx& c) {
  constexpr int NU1 = K ? 2 : 4, NU2 = K ? 3 : 1;
  const int tid = threadIdx.x, lane = c.lane, w = c.w, rt = c.rt, ct = c.ct;
  const int wm = w & 3, wn = w >> 2, r = lane & 31, hh = lane >> 5;
  const int blk = (lane >> 4) & 1, q4 = (lane & 15) >> 2, p4 = lane & 3;
  const int tb = rt * 4 + wm, c16 = (K ? 8 : 0) + ct;
  {
    const int row = 16 * w + (lane >> 3), ch = (lane & 7) ^ ((row >> 1) & 7);
    c.vx0 = row * LD_X + ch * 16;
    c.vx1 = (row + 8) * LD_X + (ch ^ 4) * 16;
    issue_x(c, 0, 0); issue_x(c, 0, 1);
    const int wrow = 8 * w + (lane >> 3), wch = (lane & 7) ^ (((lane >> 4) & 1) << 2);
    c.vw2 = (lane >> 3) * LD_W2 + wch * 16;
    c.vwq = wrow * LD_QKV + wch * 16;
    c.vwg = wrow * LD_GU + wch * 16;
    c.vmix = (((wch >> 1) & 1) ? p.wu : p.wg) + wrow * LD_GU + (1536 + 32 * ct + 16 * (wch >> 2) + 8 * (wch & 1)) * 2;
  }
#pragma unroll
  for (int u = 0; u < NU1; ++u) issue_w<K>(c, 0, u);
  load8_sc1(c.epoch, 0, c.sws, WS_EPOCH);
  load16(c.srow, lane * 16, c.sx, c.xrow + (c16 * 8 + w) * LD_X);
  dma4(L_PHI + w * 256, tid * 4, make_srd(p.wphi, 32 * 512), ct * 2048);
#pragma unroll
  for (int i = 0; i < 4; ++i) {
    const int s = (i + wm) & 3;
    c.xa[i] = L_X + (32 * wm + r) * 128 + (((2 * s + hh) ^ ((r >> 1) & 7)) * 16);
    c.wa[0][i] = L_W + 2048 * s + (8 * hh + q4) * 128 + ((wn ^ (q4 >> 1)) * 64) + 32 * blk + 8 * p4;
    c.wa[1][i] = c.wa[0][i] + 49152;
    pin(c.xa[i]); pin(c.wa[0][i]); pin(c.wa[1][i]);
  }
  __builtin_amdgcn_s_barrier();
  issue_x(c, 1, 0); issue_x(c, 1, 1);
#pragma unroll
  for (int u = 0; u < NU1; ++u) issue_w<K>(c, 1, u);
  open_stage<K, 0>(c);
  STAMP(1);

  u32x4 xf[32];
  f32x16 acc[NU1], a2[NU2];
  float cs[NU1], cs2[NU2];
  gemm_pass<K, NU1, 0, 1>(c, xf, acc, cs, c16, [](auto) {});
  STAMP(2);

  put_colsum<NU1>(cs, wm, wn, r);
  if (!K) wait_vm<8>();
  pin(c.stat);
  for (unsigned spin = 0; spin < SPIN_CAP && !__all(c.stat[1] == c.epoch[0] && c.stat[3] == c.epoch[0]); ++spin) {
    load16_sc1<0>(c.stat, (32 * wm + r) * 16, c.sws, WS_STAT + rt * 2048);
    wait_for<0>(c.stat);
  }
  const float rstd = asf(c.stat[2]), r2 = rstd * rstd;
  u32x4 negm;
  {
    const float nm = -asf(c.stat[0]);
    const float mhi = bf_lo(pack2(nm, 0.f));
    const float mm = hh ? (nm - mhi) : mhi;
    const unsigned m2 = pack2(mm, mm);
    negm = u32x4{m2, m2, m2, m2};
  }
  const f32x16 zero16 = {};
  auto logits = [&](const f32x16& a) {
    const unsigned pa = L_PHI + (2 * wn + blk) * 512 + (4 * hh + q4) * 32 + 8 * p4;
    const u32x4 wp = lds_tr2(pa, pa + 256), z = {0u, 0u, 0u, 0u};
    const f32x16 t = mfma32(blk ? z : wp, pack8(a, 0, rstd), zero16);
    return mfma32(blk ? wp : z, pack8(a, 8, rstd), t);
  };
  wg_barrier();
  apply_mean<NU1>(acc, negm, wn, r);

  if constexpr (K) {
    const unsigned img = hh * 2176 + (32 * wm + r) * 16;
    issue_w<K>(c, 10, 0);
    const f32x16 pl = logits(acc[0]);
    issue_w<K>(c, 10, 1);
#pragma unroll
    for (int s = 0; s < 2; ++s) lds_w128(L_V + (2 * wn + s) * 4352 + img, pack8(acc[1], 8 * s, rstd));
    issue_w<K>(c, 10, 2);
#pragma unroll
    for (int s = 0; s < 2; ++s) {
      u32x4 pos, neg;
      phi_k(pl, 8 * s, pos, neg);
      lds_w128(L_PK + (2 * wn + s) * 4352 + img, pos);
      lds_w128(L_PK + (4 + 2 * wn + s) * 4352 + img, neg);
      issue_w2(c, 6 + s);
    }
    issue_w2(c, 8);
    wg_barrier();
    STAMP(3);
    const unsigned kvp = WS_KVP + (ct * 16 + rt) * KVP_BYTES;
    const unsigned t = (p4 >> 1) * 2176 + (4 * (lane >> 4) + q4) * 16 + (p4 & 1) * 8;
    const u32x4 ones = {ONES2, ONES2, ONES2, ONES2};
    f32x4 kv = {}, z = {};
#pragma unroll
    for (int ks = 0; ks < 4; ++ks) {
      const unsigned ap = L_PK + w * 4352 + t + ks * 512, av = L_V + (w & 3) * 4352 + t + ks * 512;
      const u32x4 fa = lds_tr2(ap, ap + 256), fb = lds_tr2(av, av + 256);
      kv = mfma16(fa, fb, kv);
      z = mfma16(fa, ones, z);
    }
    store8_sc1(u32x2{pack2(kv[0], kv[1]), pack2(kv[2], kv[3])}, tid * 8, c.sws, kvp);
    asm volatile("s_nop 7" : "+v"(z));
    store16_sc1(__builtin_bit_cast(u32x4, z), (lane & 15) == 0 ? (lane >> 4) * 16 : OOB, c.sws, kvp + 4096 + w * 64);
    STAMP(4);
    gemm_pass<K, NU2, 8, 1>(c, xf, a2, cs2, c16, [](auto) {});
    STAMP(6);
    put_colsum<NU2>(cs2, wm, wn, r);
    wg_barrier();
    STAMP(8);
    apply_mean<NU2>(a2, negm, wn, r);
    issue_w2(c, 15); issue_w2(c, 16); issue_w2(c, 17);
    store_h(c, tb, 64 + 4 * ct + 2 * wn, a2[0], a2[1], r2);
    f32x16 h;
#pragma unroll
    for (int e = 0; e < 8; ++e) h[e] = a2[2][e] * a2[2][8 + e];
    store_frag(c, tb, 96 + 2 * ct + wn, pack8(h, 0, r2));
    wait_vm<6>();
  } else {
    issue_w2(c, 0);
    store_h(c, tb, 8 * ct + 2 * wn, acc[0], acc[1], r2);
    issue_w2(c, 1);
    store_h(c, tb, 8 * ct + 4 + 2 * wn, acc[2], acc[3], r2);
    wait_vm<6>();
    wg_barrier();
    STAMP(4);
    gemm_pass<K, NU2, 8, 3>(c, xf, a2, cs2, c16, [&](auto ic) {
      constexpr int idx = decltype(ic)::value;
      if constexpr (idx < 16 && (idx & 1)) issue_w2(c, 2 + idx / 2);
      if constexpr (idx == 20) flag_load(c, WS_FKV + ct * 128, 16);
    });
    STAMP(6);
    put_colsum<NU2>(cs2, wm, wn, r);
    wait_vm<0>();
    flag_check(c, WS_FKV + ct * 128, 16);
    const unsigned kvp = WS_KVP + ct * 16 * KVP_BYTES;
    u32x4 part[8], zpart;
    wg_barrier();
    flag_store(c, WS_FE + rt * 128 + ct * 8);
    apply_mean<NU2>(a2, negm, wn, r);
#pragma unroll
    for (int i = 0; i < 5; ++i) load16_sc1<0>(part[i], (lane & 1) * KVP_BYTES + (tid >> 1) * 16, c.sws, kvp + 2 * i * KVP_BYTES);
    const f32x16 pl = logits(a2[0]);
#pragma unroll
    for (int i = 5; i < 8; ++i) load16_sc1<0>(part[i], (lane & 1) * KVP_BYTES + (tid >> 1) * 16, c.sws, kvp + 2 * i * KVP_BYTES);
    load16_sc1<0>(zpart, (lane & 15) * KVP_BYTES + (lane >> 4) * 16, c.sws, kvp + 4096 + w * 64);
    u32x4 fq[2][2];
    float eps[2];
    phi_q(pl, 0, fq[0][0], fq[0][1], eps[0]);
    pin(fq[0][0]); pin(fq[0][1]);
    issue_w2(c, 10); issue_w2(c, 11); issue_w2(c, 12); issue_w2(c, 13);
    phi_q(pl, 8, fq[1][0], fq[1][1], eps[1]);
    pin(fq[1][0]); pin(fq[1][1]);
    issue_w2(c, 14); issue_w2(c, 15); issue_w2(c, 16); issue_w2(c, 17);
    flag_load(c, c.fe_off, 8);
    wait_vm<9>();
    {
#pragma unroll
      for (int i = 0; i < 8; ++i) pin(part[i]);
      pin(zpart);
      float v[8] = {};
#pragma unroll
      for (int i = 0; i < 8; ++i)
#pragma unroll
        for (int e = 0; e < 4; ++e) { v[2 * e] += bf_lo(part[i][e]); v[2 * e + 1] += bf_hi(part[i][e]); }
      float t[4];
#pragma unroll
      for (int e = 0; e < 4; ++e) {
        const float give = (lane & 1) ? v[e] : v[4 + e], keep = (lane & 1) ? v[4 + e] : v[e];
        t[e] = keep + dpp<0xB1>(give);
      }
      const int dl = lane & 15, d = 8 * ((dl & 7) >> 2) + 4 * (dl >> 3) + (dl & 3);
      *(LAS u32x2*)(L_KVT + w * 544 + d * 32 + (lane >> 4) * 8) = u32x2{pack2(t[0], t[1]), pack2(t[2], t[3])};
      const float z0 = row_sum(asf(zpart[0])), z1 = row_sum(asf(zpart[1])), z2 = row_sum(asf(zpart[2])), z3 = row_sum(asf(zpart[3]));
      if (dl == 0) *(LAS u32x2*)(L_KVT + w * 544 + 512 + (lane >> 4) * 8) = u32x2{pack2(z0, z1), pack2(z2, z3)};
    }
    wg_barrier();
    STAMP(8);
#pragma unroll
    for (int s = 0; s < 2; ++s) {
      const unsigned a = L_KVT + (2 * wn + s) * 544 + (r < 16 ? r : 16) * 32 + 16 * hh;
      f32x16 o = mfma32(lds_r128(a), fq[s][0], zero16);
      o = mfma32(lds_r128(a + 4 * 544), fq[s][1], o);
      store_frag(c, tb, 112 + 4 * ct + 2 * wn + s, pack8(o, 0, __builtin_amdgcn_rcpf(o[8] + eps[s])));
    }
    wait_vm<2>();
  }
  STAMP(9);
}

constexpr int PA = 6, RING = 8, J_FLAG = 7, J_CHECK = 11;
constexpr int frag_step(int f) { return f < PA ? -1 : f < 16 ? f - PA : f < 20 ? J_CHECK + (f - 16) / 2 : f - 7; }
constexpr int n_frag(int j) { int n = 0; for (int f = 0; f < 36; ++f) n += frag_step(f) == j; return n; }
constexpr int n_refill(int j) { return j >= 2 && j <= 19; }
constexpr int n_ops(int j) { return j < 0 ? PA + 1 : (j == J_FLAG) + n_frag(j) + n_refill(j); }
constexpr int ops_between(int a, int b) { int n = 0; for (int t = a; t < b; ++t) n += n_ops(t); return n; }
constexpr int vm_frag(int f) {
  const int s = frag_step(f);
  int n = s < 0 ? 1 : n_refill(s);
  for (int g = f + 1; g < 36; ++g) n += frag_step(g) == s;
  return n + ops_between(s + 1, f);
}
constexpr bool p2_barrier(int j) { return (j % 3 == 2 && j <= 17) || j == 27; }

DI void phase2(const Args& p, Ctx& c, int c16) {
  const int lane = c.lane, w = c.w, q = w & 3, m = w >> 2, r = lane & 31, hh = lane >> 5;
  const int blk = (lane >> 4) & 1, q4 = (lane & 15) >> 2, p4 = lane & 3;
  const unsigned va = lane * 16, sa = WS_AFRAG + (2 * c.rt2 + m) * NKS * 1024;
  const unsigned xo = (c.rt2 * 64 + m * 32 + r) * LD_X + (q * 16 + hh * 8) * 2;
  u32x4 af[RING], xin;
  flag_check(c, c.fe_off, 8);
  static_for<0, PA>([&](auto ic) { load16_sc1<0>(af[ic.value], va, c.sws, sa + p2_ks(c, ic.value) * 1024); });
  load16(xin, xo, c.sx, c.col2);
  unsigned rw[3][2];
#pragma unroll
  for (int i = 0; i < 3; ++i)
#pragma unroll
    for (int t = 0; t < 2; ++t) {
      rw[i][t] = i * 65536 + q * 2048 + (4 * hh + q4) * 128 + ((t ^ (q4 >> 1)) * 64) + 32 * blk + 8 * p4;
      pin(rw[i][t]);
    }
  auto read_w2 = [&](int j, int t) {
    const unsigned a = rw[l_step(j) >> 16][t] + (l_step(j) & 65535);
    return lds_tr2(a, a + 1024);
  };
  wait_vm<PA + 1>();
  wg_barrier();
  flag_store(c, WS_FH + c.rt * 128 + c16 * 8);
  STAMP(10);
  if (w == 1 && blockIdx.x == 0) {
    for (unsigned spin = 0; spin < SPIN_CAP; ++spin) {
      u32x4 a, b;
      load16_sc1<0>(a, lane * 32, c.sws, WS_ARR);
      load16_sc1<16>(b, lane * 32, c.sws, WS_ARR);
      wait_for<0>(a); pin(b);
      const bool ok = a[0] == c.epoch[0] && a[1] == c.epoch[1] && a[2] == c.epoch[0] && a[3] == c.epoch[1] &&
                      b[0] == c.epoch[0] && b[1] == c.epoch[1] && b[2] == c.epoch[0] && b[3] == c.epoch[1];
      if (__all(ok)) break;
    }
    store8_sc1(c.epoch, lane == 0 ? 0 : OOB, c.sws, WS_EPOCH);
  }
  const unsigned ex = m * 24576 + lane * 16;
  auto put = [&](int base, int n, const f32x16 (&a)[2]) {
    const int dst = n >> 1, o = (dst & 1) * 8 + (n & 1) * 4;
    const f32x16& t = a[dst >> 1];
    const unsigned slot = ((q - dst - 1) & 3) * 2048;
    if (q != dst) lds_w128(base + ex + dst * 6144 + slot + (n & 1) * 1024, __builtin_bit_cast(u32x4, f32x4{t[o], t[o + 1], t[o + 2], t[o + 3]}));
  };
  f32x16 accb[2] = {}, accs[2] = {};
  u32x4 wf[2][2];
  wf[0][0] = read_w2(0, 0); wf[0][1] = read_w2(0, 1);
  wf[1][0] = read_w2(1, 0); wf[1][1] = read_w2(1, 1);
  SB();
  static_for<0, 36>([&](auto ic) {
    constexpr int J = decltype(ic)::value;
    f32x16 (&acc)[2] = J < 28 ? accb : accs;
    wait_for<vm_frag(J)>(af[J % RING]);
    if constexpr (J > 28) { pin(acc[0]); pin(acc[1]); }
    SB();
    acc[0] = mfma32(wf[J & 1][0], af[J % RING], acc[0]);
    SB();
    if constexpr (p2_barrier(J + 1)) {
      if constexpr (J + 1 == 17) wait_vm<ops_between(12, J)>();
      if constexpr (J + 1 == 27) wait_vm<ops_between(20, J)>();
      wg_barrier();
      SB();
    }
    if constexpr (J + 2 < 36) wf[J & 1][0] = read_w2(J + 2, 0);
    SB();
    acc[1] = mfma32(wf[J & 1][1], af[J % RING], acc[1]);
    SB();
    if constexpr (J + 2 < 36) wf[J & 1][1] = read_w2(J + 2, 1);
    if constexpr (J == J_FLAG) flag_load(c, c.fh_off, 16);
    if constexpr (J == J_CHECK) {
      wait_vm<n_frag(J_FLAG) + n_refill(J_FLAG) + ops_between(J_FLAG + 1, J)>();
      STAMP(11);
      flag_check(c, c.fh_off, 16);
      STAMP(12);
    }
    static_for<PA, 36>([&](auto fc) {
      if constexpr (frag_step(fc.value) == J) load16_sc1<0>(af[fc.value % RING], va, c.sws, sa + p2_ks(c, fc.value) * 1024);
    });
    if constexpr (n_refill(J)) issue_w2(c, J + 16);
    if constexpr (J >= 28) put(0, J - 28, accb);
    SB();
  });
  pin(xin); pin(accs[0]); pin(accs[1]);
  STAMP(13);
#pragma unroll
  for (int n = 0; n < 8; ++n) put(49152, n, accs);
  float b[8], sv[8];
#pragma unroll
  for (int e = 0; e < 8; ++e) {
    const float b0 = (q & 1) ? accb[0][8 + e] : accb[0][e], b1 = (q & 1) ? accb[1][8 + e] : accb[1][e];
    const float s0 = (q & 1) ? accs[0][8 + e] : accs[0][e], s1 = (q & 1) ? accs[1][8 + e] : accs[1][e];
    b[e] = (q & 2) ? b1 : b0;
    sv[e] = (q & 2) ? s1 : s0;
  }
  wg_barrier();
#pragma unroll
  for (int s = 0; s < 3; ++s) {
    const unsigned a = ex + q * 6144 + s * 2048;
    const f32x4 t0 = __builtin_bit_cast(f32x4, lds_r128(a)), t1 = __builtin_bit_cast(f32x4, lds_r128(a + 1024));
    const f32x4 t2 = __builtin_bit_cast(f32x4, lds_r128(a + 49152)), t3 = __builtin_bit_cast(f32x4, lds_r128(a + 50176));
#pragma unroll
    for (int e = 0; e < 4; ++e) { b[e] += t0[e]; b[4 + e] += t1[e]; sv[e] += t2[e]; sv[4 + e] += t3[e]; }
  }
  float y[8];
#pragma unroll
  for (int e = 0; e < 8; ++e) {
    const float sg = __builtin_amdgcn_rcpf(1.f + __builtin_amdgcn_exp2f(b[e] * -1.44269504088896f));
    y[e] = sv[e] * (b[e] * sg);
  }
#pragma unroll
  for (int e = 0; e < 4; ++e) {
    const auto t = __builtin_amdgcn_permlane32_swap(asu(y[e]), asu(y[4 + e]), false, false);
    y[e] = asf(t[0]);
    y[4 + e] = asf(t[1]);
  }
  u32x4 o;
#pragma unroll
  for (int e = 0; e < 4; ++e) o[e] = pack2(bf_lo(xin[e]) + y[2 * e], bf_hi(xin[e]) + y[2 * e + 1]);
  *(GAS u32x4*)(p.out + xo + c.col2) = o;
  STAMP(14);
}

__global__ __launch_bounds__(512, 2) void hh_fused_kernel(Args p) {
  extern __shared__ __attribute__((aligned(16))) char smem[];
  Ctx c;
  STAMP(0);
  c.lane = threadIdx.x & 63;
  c.w = __builtin_amdgcn_readfirstlane(threadIdx.x >> 6);
  const int xcd = blockIdx.x & 7, j = blockIdx.x >> 3;
  c.rt = 8 * (xcd & 1) + (j & 7);
  c.ct = 2 * (xcd >> 1) + (j >> 4);
  c.rt2 = 4 * xcd + (j & 3);
  c.col2 = (j >> 2) * 128;
  c.xrow = c.rt * 128 * LD_X;
  c.sx = make_srd(p.x, 2048 * LD_X);
  c.sws = make_srd(p.ws, WS_BYTES);
  c.sd = make_srd(p.wd, 1792 * LD_W2);
  c.so = make_srd(p.wo, 512 * LD_W2);
  c.sq = make_srd(p.wqkv, 512 * LD_QKV);
  c.sg = make_srd(p.wg, 512 * LD_GU);
  c.su = make_srd(p.wu, 512 * LD_GU);
  if (CODE_PF) {
    u64 pc;
    asm volatile("s_getpc_b64 %0" : "=s"(pc));
    const int line = j * 16 + (c.lane & 15);
    dma4(L_SINK, (c.w == 7 && c.lane < 16) ? line * 128 : OOB, make_srd((const char*)(pc & ~255ull), CODE_BYTES), 0);
  }
  c.fe_off = WS_FE + (c.rt2 >> 1) * 128;
  c.fh_off = WS_FH + (c.rt2 >> 1) * 128;
  asm volatile("" : "+s"(c.fe_off), "+s"(c.fh_off));
  c.rot1 = ROT1 ? 4 * ((j ^ (j >> 3) ^ (j >> 4)) & 1) : 0;
  c.rot2 = ROT2 ? ((j >> 2) + 2 * (j & 3)) & 7 : 0;
  if ((j >> 3) & 1) phase1<true>(p, c);
  else phase1<false>(p, c);
  phase2(p, c, ((j >> 3) & 1) * 8 + c.ct);
  STAMP_DUMP();
}

extern "C" void kernel_launch(void* const* d_in, const int* in_sizes, int n_in, void* d_out, int out_size,
                              void* d_ws, size_t ws_size, hipStream_t stream) {
  Args a;
  a.x = (const char*)d_in[0]; a.wqkv = (const char*)d_in[1]; a.wphi = (const char*)d_in[2]; a.wo = (const char*)d_in[3];
  a.wg = (const char*)d_in[5]; a.wu = (const char*)d_in[6]; a.wd = (const char*)d_in[7];
  a.ws = (char*)d_ws; a.out = (char*)d_out;
  (void)hipFuncSetAttribute((const void*)hh_fused_kernel, hipFuncAttributeMaxDynamicSharedMemorySize, LDS_BYTES);
  hh_fused_kernel<<<256, 512, LDS_BYTES, stream>>>(a);
}
```

```cpp
#include <hip/hip_runtime.h>
#include <hip/hip_bf16.h>
#include <stdint.h>

typedef unsigned long long u64;
typedef __attribute__((ext_vector_type(8))) __bf16 b16x8;
typedef __attribute__((ext_vector_type(2))) __bf16 b16x2;
typedef __attribute__((ext_vector_type(4))) short s16x4;
typedef __attribute__((ext_vector_type(2))) float f32x2;
typedef __attribute__((ext_vector_type(4))) float f32x4;
typedef __attribute__((ext_vector_type(16))) float f32x16;
typedef __attribute__((ext_vector_type(2))) unsigned u32x2;
typedef __attribute__((ext_vector_type(4))) unsigned u32x4;
typedef __attribute__((ext_vector_type(4))) int i32x4;
#define LAS __attribute__((address_space(3)))
#define GAS __attribute__((address_space(1)))
#define DI __device__ __forceinline__
#define SB() __builtin_amdgcn_sched_barrier(0)

constexpr int LD_X = 1024, LD_QKV = 3072, LD_GU = 3584, LD_W2 = 1024;
constexpr int NKS = 144;
constexpr unsigned WS_AFRAG = 0;
constexpr unsigned WS_KVP = 64u * NKS * 1024;
constexpr unsigned KVP_BYTES = 4608;
constexpr unsigned WS_STAT = WS_KVP + 8 * 16 * KVP_BYTES;
constexpr unsigned WS_FKV = WS_STAT + 2048 * 16;
constexpr unsigned WS_FE = WS_FKV + 8 * 128;
constexpr unsigned WS_FH = WS_FE + 16 * 128;
constexpr unsigned WS_ARR = WS_FH + 16 * 128;
constexpr unsigned WS_EPOCH = WS_ARR + 256 * 8;
constexpr unsigned WS_BYTES = WS_EPOCH + 128;
constexpr unsigned OOB = 0x7ffffff0u;
constexpr unsigned SPIN_CAP = 1u << 20;

constexpr int L_X = 0;
constexpr int L_PK = 0;
constexpr int L_V = 8 * 4352;
constexpr int L_PHI = 53248;
constexpr int L_CS = 55296;
constexpr int L_SINK = 59392;
constexpr int L_KVT = 60416;
constexpr int L_W = 65536;
constexpr int LDS_BYTES = 163840;
template <bool K> constexpr int l_stage(int g) {
  if (K) return g < 8 ? (g % 3) * 16384 : (g - 8) % 3 == 2 ? 24576 : 49152 + ((g - 8) % 3) * 24576;
  return g < 8 ? ((g + 2) % 3) * 32768 : 32768 + (g - 8) * 8192;
}
constexpr int l_step(int j) { return (j % 18 < 6 ? j % 18 : j % 18 + 2) * 8192; }

#define STAMP(n)
#define STAMP_DUMP()

#define CODE_PF 1
#define CODE_BYTES 46000
#define PAGE_PF 2
#define ROT1 0
#define ROT2 1
struct Args {
  const char* x; const char* wqkv; const char* wphi; const char* wo;
  const char* wg; const char* wu; const char* wd;
  char* ws; char* out;
};

DI i32x4 make_srd(const char* p, unsigned bytes) {
  const u64 a = (u64)p;
  i32x4 r;
  r[0] = __builtin_amdgcn_readfirstlane((int)(unsigned)a);
  r[1] = __builtin_amdgcn_readfirstlane((int)(unsigned)(a >> 32) & 0xffff);
  r[2] = (int)bytes;
  r[3] = 0x00020000;
  return r;
}
DI i32x4 sel_srd(bool first, i32x4 a, i32x4 b) {
  i32x4 r;
  const int a0 = a[0], a1 = a[1], a2 = a[2], b0 = b[0], b1 = b[1], b2 = b[2];
  r[0] = first ? a0 : b0; r[1] = first ? a1 : b1; r[2] = first ? a2 : b2; r[3] = 0x00020000;
  return r;
}
DI void dma16(unsigned lds, unsigned voff, i32x4 srd, unsigned soff) {
  asm volatile("s_mov_b32 m0, %0\n\ts_nop 0\n\tbuffer_load_dwordx4 %1, %2, %3 offen lds"
               :: "s"(lds), "v"(voff), "s"(srd), "s"(soff) : "memory");
}
DI void dma4(unsigned lds, unsigned voff, i32x4 srd, unsigned soff) {
  asm volatile("s_mov_b32 m0, %0\n\ts_nop 0\n\tbuffer_load_dword %1, %2, %3 offen lds"
               :: "s"(lds), "v"(voff), "s"(srd), "s"(soff) : "memory");
}
DI void dma16g(unsigned lds, const char* gptr) {
  asm volatile("s_mov_b32 m0, %0\n\ts_nop 0\n\tglobal_load_lds_dwordx4 %1, off" :: "s"(lds), "v"(gptr) : "memory");
}
template <int IMM> DI void load16_sc1(u32x4& d, unsigned voff, i32x4 srd, unsigned soff) {
  asm volatile("buffer_load_dwordx4 %0, %1, %2, %3 offen offset:%4 sc1" : "=v"(d) : "v"(voff), "s"(srd), "s"(soff), "n"(IMM) : "memory");
}
DI void load16(u32x4& d, unsigned voff, i32x4 srd, unsigned soff) {
  asm volatile("buffer_load_dwordx4 %0, %1, %2, %3 offen" : "=v"(d) : "v"(voff), "s"(srd), "s"(soff) : "memory");
}
DI void load8_sc1(u32x2& d, unsigned voff, i32x4 srd, unsigned soff) {
  asm volatile("buffer_load_dwordx2 %0, %1, %2, %3 offen sc1" : "=v"(d) : "v"(voff), "s"(srd), "s"(soff) : "memory");
}
DI void store16_sc1(u32x4 v, unsigned voff, i32x4 srd, unsigned soff) {
  asm volatile("buffer_store_dwordx4 %0, %1, %2, %3 offen sc1\n\ts_nop 1" :: "v"(v), "v"(voff), "s"(srd), "s"(soff) : "memory");
}
DI void store8_sc1(u32x2 v, unsigned voff, i32x4 srd, unsigned soff) {
  asm volatile("buffer_store_dwordx2 %0, %1, %2, %3 offen sc1\n\ts_nop 1" :: "v"(v), "v"(voff), "s"(srd), "s"(soff) : "memory");
}
template <int N> DI void wait_vm() { asm volatile("s_waitcnt vmcnt(%0)" :: "n"(N) : "memory"); }
template <int N, class T> DI void wait_for(T& v) { asm volatile("s_waitcnt vmcnt(%1)" : "+v"(v) : "n"(N) : "memory"); }
template <class T> DI void pin(T& v) { asm volatile("" : "+v"(v)); }
DI void wg_barrier() {
  asm volatile("s_waitcnt lgkmcnt(0)" ::: "memory");
  __builtin_amdgcn_s_barrier();
  asm volatile("" ::: "memory");
}
DI u32x4 lds_r128(unsigned a) { return *(const LAS u32x4*)a; }
DI void lds_w128(unsigned a, u32x4 v) { *(LAS u32x4*)a = v; }
DI u32x4 lds_tr2(unsigned a0, unsigned a1) {
  const s16x4 lo = __builtin_amdgcn_ds_read_tr16_b64_v4i16((LAS s16x4*)a0);
  const s16x4 hi = __builtin_amdgcn_ds_read_tr16_b64_v4i16((LAS s16x4*)a1);
  const u32x2 l = __builtin_bit_cast(u32x2, lo), h = __builtin_bit_cast(u32x2, hi);
  return u32x4{l[0], l[1], h[0], h[1]};
}
DI f32x16 mfma32(u32x4 a, u32x4 b, f32x16 c) {
  return __builtin_amdgcn_mfma_f32_32x32x16_bf16(__builtin_bit_cast(b16x8, a), __builtin_bit_cast(b16x8, b), c, 0, 0, 0);
}
DI f32x4 mfma16(u32x4 a, u32x4 b, f32x4 c) {
  return __builtin_amdgcn_mfma_f32_16x16x32_bf16(__builtin_bit_cast(b16x8, a), __builtin_bit_cast(b16x8, b), c, 0, 0, 0);
}
DI unsigned pack2(float lo, float hi) {
  const b16x2 v = __builtin_convertvector(f32x2{lo, hi}, b16x2);
  return __builtin_bit_cast(unsigned, v);
}
DI float asf(unsigned v) { return __builtin_bit_cast(float, v); }
DI unsigned asu(float v) { return __builtin_bit_cast(unsigned, v); }
DI float bf_lo(unsigned v) { return asf(v << 16); }
DI float bf_hi(unsigned v) { return asf(v & 0xffff0000u); }
DI float half_sum(float v) {
  const auto r = __builtin_amdgcn_permlane32_swap(asu(v), asu(v), false, false);
  return asf(r[0]) + asf(r[1]);
}
DI float half_max(float v) {
  const auto r = __builtin_amdgcn_permlane32_swap(asu(v), asu(v), false, false);
  return fmaxf(asf(r[0]), asf(r[1]));
}
template <int CTRL> DI float dpp(float v) {
  return asf((unsigned)__builtin_amdgcn_update_dpp(0, (int)asu(v), CTRL, 0xf, 0xf, true));
}
DI float row_sum(float s) {
  s += dpp<0xB1>(s); s += dpp<0x4E>(s); s += dpp<0x141>(s); s += dpp<0x140>(s);
  return s;
}
DI float wave_sum(float s) {
  s = row_sum(s);
  const auto r = __builtin_amdgcn_permlane16_swap(asu(s), asu(s), false, false);
  return half_sum(asf(r[0]) + asf(r[1]));
}
DI u32x4 pack8(const f32x16& a, int o, float s) {
  return u32x4{pack2(a[o] * s, a[o + 1] * s), pack2(a[o + 2] * s, a[o + 3] * s),
               pack2(a[o + 4] * s, a[o + 5] * s), pack2(a[o + 6] * s, a[o + 7] * s)};
}
constexpr unsigned ONES2 = 0x3f803f80u;
template <int V> struct IC { static constexpr int value = V; };
template <int I, int N, class F> DI void static_for(F&& f) {
  if constexpr (I < N) { f(IC<I>{}); static_for<I + 1, N>(f); }
}

DI float max3(float a, float b, float c) { float r; asm("v_max3_f32 %0, %1, %2, %3" : "=v"(r) : "v"(a), "v"(b), "v"(c)); return r; }
DI float nmax3(float a, float b, float c) { float r; asm("v_max3_f32 %0, %1, -%2, -%3" : "=v"(r) : "v"(a), "v"(b), "v"(c)); return r; }
DI float max2(float a, float b) { float r; asm("v_max_f32 %0, %1, %2" : "=v"(r) : "v"(a), "v"(b)); return r; }
template <class F> DI float fold2(float a, float b, F op) {
  const auto r = __builtin_amdgcn_permlane32_swap(asu(a), asu(b), false, false);
  return op(asf(r[0]), asf(r[1]));
}
DI void spread2(float v, float& a, float& b) {
  const auto r = __builtin_amdgcn_permlane32_swap(asu(v), asu(v), false, false);
  a = asf(r[0]); b = asf(r[1]);
}
DI void dual_exp(const f32x16& p, int o, f32x2 (&ep)[4], f32x2 (&en)[4], float& sp, float& sn) {
  const float L2E = 1.44269504088896f;
  const float mx = max2(max3(max3(max3(p[o], p[o + 1], p[o + 2]), p[o + 3], p[o + 4]), p[o + 5], p[o + 6]), p[o + 7]);
  const float nm = nmax3(nmax3(nmax3(nmax3(-3.0e38f, p[o], p[o + 1]), p[o + 2], p[o + 3]), p[o + 4], p[o + 5]), p[o + 6], p[o + 7]);
  float a, b;
  spread2(fold2(mx, nm, max2) * -L2E, a, b);
  const f32x2 l2 = {L2E, L2E}, a2 = {a, a}, b2 = {b, b};
#pragma unroll
  for (int i = 0; i < 4; ++i) {
    const f32x2 v = {p[o + 2 * i], p[o + 2 * i + 1]};
    const f32x2 tp = __builtin_elementwise_fma(v, l2, a2), tn = __builtin_elementwise_fma(-v, l2, b2);
    ep[i] = f32x2{__builtin_amdgcn_exp2f(tp[0]), __builtin_amdgcn_exp2f(tp[1])};
    en[i] = f32x2{__builtin_amdgcn_exp2f(tn[0]), __builtin_amdgcn_exp2f(tn[1])};
  }
  const f32x2 s2 = (ep[0] + ep[1]) + (ep[2] + ep[3]), t2 = (en[0] + en[1]) + (en[2] + en[3]);
  sp = s2[0] + s2[1]; sn = t2[0] + t2[1];
}
DI u32x4 pack8s(const f32x2 (&e)[4], float s) {
  u32x4 r;
#pragma unroll
  for (int i = 0; i < 4; ++i) { const f32x2 t = e[i] * s; r[i] = pack2(t[0], t[1]); }
  return r;
}
DI void phi_k(const f32x16& p, int o, u32x4& pos, u32x4& neg) {
  f32x2 ep[4], en[4];
  float sp, sn, rp, rn;
  dual_exp(p, o, ep, en, sp, sn);
  spread2(__builtin_amdgcn_rcpf(fold2(sp, sn, [](float x, float y) { return x + y; })), rp, rn);
  pos = pack8s(ep, rp);
  neg = pack8s(en, rn);
}
DI void phi_q(const f32x16& p, int o, u32x4& pos, u32x4& neg, float& eps) {
  f32x2 ep[4], en[4];
  float sp, sn;
  dual_exp(p, o, ep, en, sp, sn);
  spread2(fold2(sp, sn, [](float x, float y) { return x + y; }), sp, sn);
#pragma unroll
  for (int i = 0; i < 4; ++i) pos[i] = pack2(ep[i][0], ep[i][1]);
  neg = pack8s(en, sp * __builtin_amdgcn_rcpf(sn));
  eps = sp * 1e-6f;
}

enum Op { OP_NONE, OP_X0, OP_X1, OP_W0, OP_W1, OP_W2, OP_W3, OP_TOUCH0, OP_TOUCH1, OP_TOUCH2, OP_STAT, OP_P2W, OP_FE };
struct Slot { int op, arg; };
constexpr int NEVER = 99, N_SLOT = 7;
template <bool K> constexpr Slot slot_at(int t, int n) {
  constexpr int NU = K ? 2 : 4;
  if (t < 8) {
    if (n == 0) return t == 0 ? Slot{OP_TOUCH0, 0} : t == 2 ? Slot{OP_TOUCH1, 0} : t == 4 ? Slot{OP_TOUCH2, 0} : t == 6 ? Slot{OP_STAT, 0} : Slot{OP_NONE, 0};
    if (t < 6) return n < 3 ? Slot{OP_X0 + n - 1, t + 2} : n - 3 < NU ? Slot{OP_W0 + n - 3, t + 2} : Slot{OP_NONE, 0};
    if (K) return n < 4 ? Slot{OP_W0 + n - 1, t + 2} : Slot{OP_NONE, 0};
    return n < 5 ? Slot{OP_W0, 8 + 4 * (t - 6) + n - 1} : Slot{OP_NONE, 0};
  }
  if (!K || t == 8) return Slot{OP_NONE, 0};
  if (t <= 13) return n < 3 ? Slot{OP_W0 + n, t + 2} : n == 3 ? Slot{OP_P2W, t - 9} : (n == 4 && t == 13) ? Slot{OP_P2W, 5} : Slot{OP_NONE, 0};
  if (n < 3) return Slot{OP_P2W, 9 + 3 * (t - 14) + n};
  return (n == 3 && t == 15) ? Slot{OP_FE, 0} : Slot{OP_NONE, 0};
}
template <bool K> constexpr int op_needed_by(Slot s) {
  if (s.op >= OP_X0 && s.op <= OP_W3) return (!K && s.arg > 8) ? 8 : s.arg;
  return s.op == OP_STAT ? 8 : NEVER;
}
template <bool K> constexpr int vm_at(int g) {
  if (g == 0) return K ? 4 : 6;
  if (g == 9) return 8;
  int n = 0;
  for (int t = g - 1; t >= 0; --t) {
    if (t == 8 && g >= 10) return n;
    for (int i = N_SLOT - 1; i >= 0; --i) {
      const Slot s = slot_at<K>(t, i);
      if (s.op != OP_NONE && op_needed_by<K>(s) <= g) return n;
      n += s.op != OP_NONE;
    }
  }
  return n;
}

struct Ctx {
  i32x4 sx, sq, sg, su, sws, sd, so;
  unsigned vx0, vx1, vwq, vwg, vw2;
  const char* vmix;
  unsigned xrow;
  unsigned col2;
  int w, lane, rt, ct, rt2;
  unsigned fe_off, fh_off;
  int rot1, rot2;
  unsigned xa[4];
  unsigned wa[2][4];
  u32x2 epoch, flag;
  u32x4 srow, stat;
};

DI int k_slice(const Ctx& c, int g) { return (g + c.rot1) & 7; }
DI void issue_x(const Ctx& c, int j, int n) {
  dma16(L_X + (j % 3) * 16384 + c.w * 2048 + n * 1024, n ? c.vx1 + 0u : c.vx0 + 0u, c.sx, c.xrow + 128 * k_slice(c, j));
}
template <bool K> DI void issue_w(const Ctx& c, int g, int u) {
  const unsigned l = L_W + l_stage<K>(g) + u * 8192 + c.w * 1024;
  const unsigned k0 = k_slice(c, g) * 64;
  if (K ? g < 8 : g >= 8) dma16(l, c.vwq, c.sq, c.ct * 128 + k0 * LD_QKV + (K ? 1024 + u * 1024 : 0));
  else if (K && u == 2) dma16g(l, c.vmix + k0 * LD_GU);
  else dma16(l, c.vwg, sel_srd(u & 1, c.su, c.sg), k0 * LD_GU + (K ? 2048 + c.ct * 128 : c.ct * 256 + (u >> 1) * 128));
}
DI int p2_ks(const Ctx& c, int j) {
  const int q = c.w & 3;
  if (j < 16) return 16 * q + ((j + 2 * c.rot2) & 15);
  if (j >= 28) return 112 + 8 * q + ((j - 28 + c.rot2) & 7);
  const int t = j - 16 + ((3 * c.rot2) >> 1);
  return 64 + 12 * q + (t >= 12 ? t - 12 : t);
}
DI void issue_w2(const Ctx& c, int j) {
  const int q = c.w & 3, m = c.w >> 2;
  dma16(l_step(j) + q * 2048 + m * 1024, c.vw2, sel_srd(j >= 28, c.so, c.sd),
        c.col2 + ((p2_ks(c, j) - (j >= 28 ? 112 : 0)) * 16 + m * 8) * LD_W2);
}
template <bool K> DI void touch(const Ctx& c, int which) {
  const unsigned sink = L_SINK + (c.w & 3) * 256;
  const int odd = c.w & 1, hw = c.w >> 1, l = c.lane;
  if (which == 2) {
    const bool wo = c.w == 7;
    const int row = wo ? 16 * c.rt2 + l : 56 * c.rt2 + 8 * c.w + l;
    dma4(sink, l < (wo ? 16 : 8) ? row * LD_W2 : OOB, sel_srd(wo, c.so, c.sd), c.col2);
  } else if (K == (which == 0)) {
    const int row = 32 * c.rt + hw * 8 + l;
    dma4(sink, (l < 8 && (K || !odd)) ? row * LD_QKV : OOB, c.sq, c.ct * 128 + (K ? 1024 + odd * 1024 : 0));
  } else {
    const int row = 32 * c.rt + hw * 8 + (l >> 1);
    const unsigned cb = K ? ((l & 1) ? 3072 + 64 * c.ct : 2048 + 128 * c.ct) : 256 * c.ct + (l & 1) * 128;
    dma4(sink, l < 16 ? row * LD_GU + cb : OOB, sel_srd(odd, c.su, c.sg), 0);
  }
}
DI void flag_load(Ctx& c, unsigned off, int n) { load8_sc1(c.flag, (c.lane & (n - 1)) * 8, c.sws, off); }
DI void flag_check(Ctx& c, unsigned off, int n) {
  pin(c.flag);
  for (unsigned spin = 0; spin < SPIN_CAP && !__all(c.flag[0] == c.epoch[0] && c.flag[1] == c.epoch[1]); ++spin) {
    flag_load(c, off, n);
    wait_for<0>(c.flag);
  }
}
DI void flag_store(const Ctx& c, unsigned off) {
  if (c.w == 0) store8_sc1(c.epoch, c.lane == 0 ? 0 : OOB, c.sws, off);
}
template <bool K, int T, int N> DI void run_slot(Ctx& c) {
  constexpr Slot s = slot_at<K>(T, N);
  if constexpr (s.op == OP_X0 || s.op == OP_X1) issue_x(c, s.arg, s.op - OP_X0);
  if constexpr (s.op >= OP_W0 && s.op <= OP_W3) issue_w<K>(c, s.arg, s.op - OP_W0);
  if constexpr (s.op >= OP_TOUCH0 && s.op <= OP_TOUCH2) touch<K>(c, s.op - OP_TOUCH0);
  if constexpr (s.op == OP_STAT) load16_sc1<0>(c.stat, (32 * (c.w & 3) + (c.lane & 31)) * 16, c.sws, WS_STAT + c.rt * 2048);
  if constexpr (s.op == OP_P2W) issue_w2(c, s.arg);
  if constexpr (s.op == OP_FE) flag_load(c, c.fe_off, 8);
}
template <bool K, int G> DI void open_stage(Ctx& c) {
  wait_vm<vm_at<K>(G)>(); wg_barrier();
  if (K && G == 10) flag_store(c, WS_FKV + c.ct * 128 + c.rt * 8);
}
template <bool K> DI u32x4 read_w(const Ctx& c, int g, int i, int u) {
  const int o = l_stage<K>(g) + u * 8192;
  const unsigned a = c.wa[o >= 49152][i] + o % 49152;
  return lds_tr2(a, a + 512);
}

struct RowStat { float v[8]; float s; float mean; };
DI void stat_step(Ctx& c, RowStat& t, int n, int c16) {
  if (n == 0) {
    pin(c.srow); pin(c.epoch);
#pragma unroll
    for (int e = 0; e < 4; ++e) { t.v[2 * e] = bf_lo(c.srow[e]); t.v[2 * e + 1] = bf_hi(c.srow[e]); pin(t.v[2 * e]); pin(t.v[2 * e + 1]); }
  }
  if (n == 1) { t.s = ((t.v[0] + t.v[1]) + (t.v[2] + t.v[3])) + ((t.v[4] + t.v[5]) + (t.v[6] + t.v[7])); pin(t.s); }
  if (n == 2) { t.s = row_sum(t.s); pin(t.s); }
  if (n == 3) {
    const auto r = __builtin_amdgcn_permlane16_swap(asu(t.s), asu(t.s), false, false);
    t.mean = half_sum(asf(r[0]) + asf(r[1])) * (1.f / 512.f); pin(t.mean);
  }
  if (n == 4) {
#pragma unroll
    for (int e = 0; e < 8; ++e) { t.v[e] -= t.mean; pin(t.v[e]); }
  }
  if (n == 5) {
    float a = t.v[0] * t.v[0], b = t.v[1] * t.v[1];
#pragma unroll
    for (int e = 2; e < 8; e += 2) { a = fmaf(t.v[e], t.v[e], a); b = fmaf(t.v[e + 1], t.v[e + 1], b); }
    t.s = a + b; pin(t.s);
  }
  if (n == 6) { t.s = row_sum(t.s); pin(t.s); }
  if (n == 7) {
    const auto r = __builtin_amdgcn_permlane16_swap(asu(t.s), asu(t.s), false, false);
    t.s = __builtin_amdgcn_rsqf(half_sum(asf(r[0]) + asf(r[1])) * (1.f / 512.f) + 1e-5f); pin(t.s);
  }
  if (n == 8) {
    const u64 e = (((u64)c.epoch[1] << 32) | c.epoch[0]) + 1;
    c.epoch = u32x2{(unsigned)e, (unsigned)(e >> 32)};
    store16_sc1(u32x4{asu(t.mean), c.epoch[0], asu(t.s), c.epoch[0]}, c.lane == 0 ? 0 : OOB, c.sws,
                WS_STAT + (c.rt * 128 + c16 * 8 + c.w) * 16);
    if (c.w == 0) store8_sc1(c.epoch, c.lane == 0 ? 0 : OOB, c.sws, WS_ARR + blockIdx.x * 8);
  }
}

template <bool K, int NU, int G0, int PD, class H>
DI void gemm_pass(Ctx& c, u32x4 (&xf)[32], f32x16 (&acc)[NU], float (&cs)[NU], int c16, H&& hook) {
  u32x4 wf[PD][NU];
  RowStat rs;
  if (G0 == 0) xf[0] = lds_r128(c.xa[0]);
#pragma unroll
  for (int u = 0; u < NU; ++u) {
#pragma unroll
    for (int d = 0; d < PD; ++d) wf[d][u] = read_w<K>(c, G0, d, u);
    cs[u] = 0.f;
#pragma unroll
    for (int e = 0; e < 16; ++e) acc[u][e] = 0.f;
  }
  SB();
  static_for<0, 32 * NU>([&](auto ic) {
    constexpr int idx = decltype(ic)::value / NU, u = decltype(ic)::value % NU;
    constexpr int g = G0 + idx / 4, i = idx & 3, gn = G0 + (idx + PD) / 4, in = (idx + PD) & 3;
    constexpr bool staged = K || G0 == 0, opens = i == 3 && g < (K ? 15 : 7);
    u32x4& f = wf[idx % PD][u];
    cs[u] += bf_lo(f[i]);
    cs[u] += bf_hi(f[i]);
    pin(cs[u]);
    SB();
    acc[u] = mfma32(f, xf[idx], acc[u]);
    SB();
    if constexpr (u == 0 && opens) { open_stage<K, g + 1>(c); SB(); }
    if constexpr (u == 0 && G0 == 0 && idx + 1 < 32) xf[idx + 1] = lds_r128(c.xa[(idx + 1) & 3] + (((idx + 1) / 4) % 3) * 16384);
    if constexpr (idx + PD < 32) f = read_w<K>(c, gn, in, u);
    if constexpr (G0 == 0 && u < 2 && 2 * idx + u < 9) stat_step(c, rs, 2 * idx + u, c16);
    if constexpr (staged) {
      constexpr int t = i == 3 ? g + 1 : g, pos = i == 3 ? u : NU * (i + 1) + u;
      constexpr int n = t == 0 ? pos - NU : NU == 2 ? pos - 1 : (pos & 1) ? pos / 2 : -1;
      if constexpr (t < 16 && n >= 0 && n < N_SLOT) run_slot<K, t, n>(c);
    }
    if constexpr (u == NU - 1) hook(IC<idx>{});
    SB();
  });
#pragma unroll
  for (int u = 0; u < NU; ++u) pin(acc[u]);
}

template <int NU> DI void put_colsum(const float (&cs)[NU], int wm, int wn, int r) {
#pragma unroll
  for (int u = 0; u < NU; ++u) {
    const float t = half_sum(cs[u]);
    const float hi = bf_lo(pack2(t, 0.f));
    *(LAS unsigned*)(L_CS + ((u * 2 + wn) * 32 + r) * 16 + wm * 4) = pack2(hi, t - hi);
  }
}
template <int NU> DI void apply_mean(f32x16 (&acc)[NU], u32x4 negm, int wn, int r) {
#pragma unroll
  for (int u = 0; u < NU; ++u) acc[u] = mfma32(lds_r128(L_CS + ((u * 2 + wn) * 32 + r) * 16), negm, acc[u]);
}
DI void store_frag(const Ctx& c, int tb, int ks, u32x4 v) {
  store16_sc1(v, c.lane * 16, c.sws, WS_AFRAG + (tb * NKS + ks) * 1024);
}
DI void store_h(const Ctx& c, int tb, int ks, const f32x16& g, const f32x16& u, float r2) {
  f32x16 h;
#pragma unroll
  for (int e = 0; e < 16; ++e) h[e] = g[e] * u[e];
  store_frag(c, tb, ks, pack8(h, 0, r2));
  store_frag(c, tb, ks + 1, pack8(h, 8, r2));
}

template <bool K>
DI void phase1(const Args& p, Ctx& c) {
  constexpr int NU1 = K ? 2 : 4, NU2 = K ? 3 : 1;
  const int tid = threadIdx.x, lane = c.lane, w = c.w, rt = c.rt, ct = c.ct;
  const int wm = w & 3, wn = w >> 2, r = lane & 31, hh = lane >> 5;
  const int blk = (lane >> 4) & 1, q4 = (lane & 15) >> 2, p4 = lane & 3;
  const int tb = rt * 4 + wm, c16 = (K ? 8 : 0) + ct;
  {
    const int row = 16 * w + (lane >> 3), ch = (lane & 7) ^ ((row >> 1) & 7);
    c.vx0 = row * LD_X + ch * 16;
    c.vx1 = (row + 8) * LD_X + (ch ^ 4) * 16;
    issue_x(c, 0, 0); issue_x(c, 0, 1);
    const int wrow = 8 * w + (lane >> 3), wch = (lane & 7) ^ (((lane >> 4) & 1) << 2);
    c.vw2 = (lane >> 3) * LD_W2 + wch * 16;
    c.vwq = wrow * LD_QKV + wch * 16;
    c.vwg = wrow * LD_GU + wch * 16;
    c.vmix = (((wch >> 1) & 1) ? p.wu : p.wg) + wrow * LD_GU + (1536 + 32 * ct + 16 * (wch >> 2) + 8 * (wch & 1)) * 2;
  }
#pragma unroll
  for (int u = 0; u < NU1; ++u) issue_w<K>(c, 0, u);
  load8_sc1(c.epoch, 0, c.sws, WS_EPOCH);
  load16(c.srow, lane * 16, c.sx, c.xrow + (c16 * 8 + w) * LD_X);
  dma4(L_PHI + w * 256, tid * 4, make_srd(p.wphi, 32 * 512), ct * 2048);
#pragma unroll
  for (int i = 0; i < 4; ++i) {
    const int s = (i + wm) & 3;
    c.xa[i] = L_X + (32 * wm + r) * 128 + (((2 * s + hh) ^ ((r >> 1) & 7)) * 16);
    c.wa[0][i] = L_W + 2048 * s + (8 * hh + q4) * 128 + ((wn ^ (q4 >> 1)) * 64) + 32 * blk + 8 * p4;
    c.wa[1][i] = c.wa[0][i] + 49152;
    pin(c.xa[i]); pin(c.wa[0][i]); pin(c.wa[1][i]);
  }
  __builtin_amdgcn_s_barrier();
  issue_x(c, 1, 0); issue_x(c, 1, 1);
#pragma unroll
  for (int u = 0; u < NU1; ++u) issue_w<K>(c, 1, u);
  open_stage<K, 0>(c);
  STAMP(1);

  u32x4 xf[32];
  f32x16 acc[NU1], a2[NU2];
  float cs[NU1], cs2[NU2];
  gemm_pass<K, NU1, 0, 1>(c, xf, acc, cs, c16, [](auto) {});
  STAMP(2);

  put_colsum<NU1>(cs, wm, wn, r);
  if (!K) wait_vm<8>();
  pin(c.stat);
  for (unsigned spin = 0; spin < SPIN_CAP && !__all(c.stat[1] == c.epoch[0] && c.stat[3] == c.epoch[0]); ++spin) {
    load16_sc1<0>(c.stat, (32 * wm + r) * 16, c.sws, WS_STAT + rt * 2048);
    wait_for<0>(c.stat);
  }
  const float rstd = asf(c.stat[2]), r2 = rstd * rstd;
  u32x4 negm;
  {
    const float nm = -asf(c.stat[0]);
    const float mhi = bf_lo(pack2(nm, 0.f));
    const float mm = hh ? (nm - mhi) : mhi;
    const unsigned m2 = pack2(mm, mm);
    negm = u32x4{m2, m2, m2, m2};
  }
  const f32x16 zero16 = {};
  auto logits = [&](const f32x16& a) {
    const unsigned pa = L_PHI + (2 * wn + blk) * 512 + (4 * hh + q4) * 32 + 8 * p4;
    const u32x4 wp = lds_tr2(pa, pa + 256), z = {0u, 0u, 0u, 0u};
    const f32x16 t = mfma32(blk ? z : wp, pack8(a, 0, rstd), zero16);
    return mfma32(blk ? wp : z, pack8(a, 8, rstd), t);
  };
  wg_barrier();
  apply_mean<NU1>(acc, negm, wn, r);

  if constexpr (K) {
    const unsigned img = hh * 2176 + (32 * wm + r) * 16;
    issue_w<K>(c, 10, 0);
    const f32x16 pl = logits(acc[0]);
    issue_w<K>(c, 10, 1);
#pragma unroll
    for (int s = 0; s < 2; ++s) lds_w128(L_V + (2 * wn + s) * 4352 + img, pack8(acc[1], 8 * s, rstd));
    issue_w<K>(c, 10, 2);
#pragma unroll
    for (int s = 0; s < 2; ++s) {
      u32x4 pos, neg;
      phi_k(pl, 8 * s, pos, neg);
      lds_w128(L_PK + (2 * wn + s) * 4352 + img, pos);
      lds_w128(L_PK + (4 + 2 * wn + s) * 4352 + img, neg);
      issue_w2(c, 6 + s);
    }
    issue_w2(c, 8);
    wg_barrier();
    STAMP(3);
    const unsigned kvp = WS_KVP + (ct * 16 + rt) * KVP_BYTES;
    const unsigned t = (p4 >> 1) * 2176 + (4 * (lane >> 4) + q4) * 16 + (p4 & 1) * 8;
    const u32x4 ones = {ONES2, ONES2, ONES2, ONES2};
    f32x4 kv = {}, z = {};
#pragma unroll
    for (int ks = 0; ks < 4; ++ks) {
      const unsigned ap = L_PK + w * 4352 + t + ks * 512, av = L_V + (w & 3) * 4352 + t + ks * 512;
      const u32x4 fa = lds_tr2(ap, ap + 256), fb = lds_tr2(av, av + 256);
      kv = mfma16(fa, fb, kv);
      z = mfma16(fa, ones, z);
    }
    store8_sc1(u32x2{pack2(kv[0], kv[1]), pack2(kv[2], kv[3])}, tid * 8, c.sws, kvp);
    asm volatile("s_nop 7" : "+v"(z));
    store16_sc1(__builtin_bit_cast(u32x4, z), (lane & 15) == 0 ? (lane >> 4) * 16 : OOB, c.sws, kvp + 4096 + w * 64);
    STAMP(4);
    gemm_pass<K, NU2, 8, 1>(c, xf, a2, cs2, c16, [](auto) {});
    STAMP(6);
    put_colsum<NU2>(cs2, wm, wn, r);
    wg_barrier();
    STAMP(8);
    apply_mean<NU2>(a2, negm, wn, r);
    issue_w2(c, 15); issue_w2(c, 16); issue_w2(c, 17);
    store_h(c, tb, 64 + 4 * ct + 2 * wn, a2[0], a2[1], r2);
    f32x16 h;
#pragma unroll
    for (int e = 0; e < 8; ++e) h[e] = a2[2][e] * a2[2][8 + e];
    store_frag(c, tb, 96 + 2 * ct + wn, pack8(h, 0, r2));
    wait_vm<6>();
  } else {
    issue_w2(c, 0);
    store_h(c, tb, 8 * ct + 2 * wn, acc[0], acc[1], r2);
    issue_w2(c, 1);
    store_h(c, tb, 8 * ct + 4 + 2 * wn, acc[2], acc[3], r2);
    wait_vm<6>();
    wg_barrier();
    STAMP(4);
    gemm_pass<K, NU2, 8, 3>(c, xf, a2, cs2, c16, [&](auto ic) {
      constexpr int idx = decltype(ic)::value;
      if constexpr (idx < 16 && (idx & 1)) issue_w2(c, 2 + idx / 2);
      if constexpr (idx == 20) flag_load(c, WS_FKV + ct * 128, 16);
    });
    STAMP(6);
    put_colsum<NU2>(cs2, wm, wn, r);
    wait_vm<0>();
    flag_check(c, WS_FKV + ct * 128, 16);
    const unsigned kvp = WS_KVP + ct * 16 * KVP_BYTES;
    u32x4 part[8], zpart;
    wg_barrier();
    flag_store(c, WS_FE + rt * 128 + ct * 8);
    apply_mean<NU2>(a2, negm, wn, r);
#pragma unroll
    for (int i = 0; i < 5; ++i) load16_sc1<0>(part[i], (lane & 1) * KVP_BYTES + (tid >> 1) * 16, c.sws, kvp + 2 * i * KVP_BYTES);
    const f32x16 pl = logits(a2[0]);
#pragma unroll
    for (int i = 5; i < 8; ++i) load16_sc1<0>(part[i], (lane & 1) * KVP_BYTES + (tid >> 1) * 16, c.sws, kvp + 2 * i * KVP_BYTES);
    load16_sc1<0>(zpart, (lane & 15) * KVP_BYTES + (lane >> 4) * 16, c.sws, kvp + 4096 + w * 64);
    u32x4 fq[2][2];
    float eps[2];
    phi_q(pl, 0, fq[0][0], fq[0][1], eps[0]);
    pin(fq[0][0]); pin(fq[0][1]);
    issue_w2(c, 10); issue_w2(c, 11); issue_w2(c, 12); issue_w2(c, 13);
    phi_q(pl, 8, fq[1][0], fq[1][1], eps[1]);
    pin(fq[1][0]); pin(fq[1][1]);
    issue_w2(c, 14); issue_w2(c, 15); issue_w2(c, 16); issue_w2(c, 17);
    flag_load(c, c.fe_off, 8);
    wait_vm<9>();
    {
#pragma unroll
      for (int i = 0; i < 8; ++i) pin(part[i]);
      pin(zpart);
      float v[8] = {};
#pragma unroll
      for (int i = 0; i < 8; ++i)
#pragma unroll
        for (int e = 0; e < 4; ++e) { v[2 * e] += bf_lo(part[i][e]); v[2 * e + 1] += bf_hi(part[i][e]); }
      float t[4];
#pragma unroll
      for (int e = 0; e < 4; ++e) {
        const float give = (lane & 1) ? v[e] : v[4 + e], keep = (lane & 1) ? v[4 + e] : v[e];
        t[e] = keep + dpp<0xB1>(give);
      }
      const int dl = lane & 15, d = 8 * ((dl & 7) >> 2) + 4 * (dl >> 3) + (dl & 3);
      *(LAS u32x2*)(L_KVT + w * 544 + d * 32 + (lane >> 4) * 8) = u32x2{pack2(t[0], t[1]), pack2(t[2], t[3])};
      const float z0 = row_sum(asf(zpart[0])), z1 = row_sum(asf(zpart[1])), z2 = row_sum(asf(zpart[2])), z3 = row_sum(asf(zpart[3]));
      if (dl == 0) *(LAS u32x2*)(L_KVT + w * 544 + 512 + (lane >> 4) * 8) = u32x2{pack2(z0, z1), pack2(z2, z3)};
    }
    wg_barrier();
    STAMP(8);
#pragma unroll
    for (int s = 0; s < 2; ++s) {
      const unsigned a = L_KVT + (2 * wn + s) * 544 + (r < 16 ? r : 16) * 32 + 16 * hh;
      f32x16 o = mfma32(lds_r128(a), fq[s][0], zero16);
      o = mfma32(lds_r128(a + 4 * 544), fq[s][1], o);
      store_frag(c, tb, 112 + 4 * ct + 2 * wn + s, pack8(o, 0, __builtin_amdgcn_rcpf(o[8] + eps[s])));
    }
    wait_vm<2>();
  }
  STAMP(9);
}

constexpr int PA = 6, RING = 8, J_FLAG = 7, J_CHECK = 11;
constexpr int frag_step(int f) { return f < PA ? -1 : f < 16 ? f - PA : f < 20 ? J_CHECK + (f - 16) / 2 : f - 7; }
constexpr int n_frag(int j) { int n = 0; for (int f = 0; f < 36; ++f) n += frag_step(f) == j; return n; }
constexpr int n_refill(int j) { return j >= 2 && j <= 19; }
constexpr int n_ops(int j) { return j < 0 ? PA + 1 : (j == J_FLAG) + n_frag(j) + n_refill(j); }
constexpr int ops_between(int a, int b) { int n = 0; for (int t = a; t < b; ++t) n += n_ops(t); return n; }
constexpr int vm_frag(int f) {
  const int s = frag_step(f);
  int n = s < 0 ? 1 : n_refill(s);
  for (int g = f + 1; g < 36; ++g) n += frag_step(g) == s;
  return n + ops_between(s + 1, f);
}
constexpr bool p2_barrier(int j) { return (j % 3 == 2 && j <= 17) || j == 27; }

DI void phase2(const Args& p, Ctx& c, int c16) {
  const int lane = c.lane, w = c.w, q = w & 3, m = w >> 2, r = lane & 31, hh = lane >> 5;
  const int blk = (lane >> 4) & 1, q4 = (lane & 15) >> 2, p4 = lane & 3;
  const unsigned va = lane * 16, sa = WS_AFRAG + (2 * c.rt2 + m) * NKS * 1024;
  const unsigned xo = (c.rt2 * 64 + m * 32 + r) * LD_X + (q * 16 + hh * 8) * 2;
  u32x4 af[RING], xin;
  flag_check(c, c.fe_off, 8);
  static_for<0, PA>([&](auto ic) { load16_sc1<0>(af[ic.value], va, c.sws, sa + p2_ks(c, ic.value) * 1024); });
  load16(xin, xo, c.sx, c.col2);
  unsigned rw[3][2];
#pragma unroll
  for (int i = 0; i < 3; ++i)
#pragma unroll
    for (int t = 0; t < 2; ++t) {
      rw[i][t] = i * 65536 + q * 2048 + (4 * hh + q4) * 128 + ((t ^ (q4 >> 1)) * 64) + 32 * blk + 8 * p4;
      pin(rw[i][t]);
    }
  auto read_w2 = [&](int j, int t) {
    const unsigned a = rw[l_step(j) >> 16][t] + (l_step(j) & 65535);
    return lds_tr2(a, a + 1024);
  };
  wait_vm<PA + 1>();
  wg_barrier();
  flag_store(c, WS_FH + c.rt * 128 + c16 * 8);
  STAMP(10);
  if (w == 1 && blockIdx.x == 0) {
    for (unsigned spin = 0; spin < SPIN_CAP; ++spin) {
      u32x4 a, b;
      load16_sc1<0>(a, lane * 32, c.sws, WS_ARR);
      load16_sc1<16>(b, lane * 32, c.sws, WS_ARR);
      wait_for<0>(a); pin(b);
      const bool ok = a[0] == c.epoch[0] && a[1] == c.epoch[1] && a[2] == c.epoch[0] && a[3] == c.epoch[1] &&
                      b[0] == c.epoch[0] && b[1] == c.epoch[1] && b[2] == c.epoch[0] && b[3] == c.epoch[1];
      if (__all(ok)) break;
    }
    store8_sc1(c.epoch, lane == 0 ? 0 : OOB, c.sws, WS_EPOCH);
  }
  const unsigned ex = m * 24576 + lane * 16;
  auto put = [&](int base, int n, const f32x16 (&a)[2]) {
    const int dst = n >> 1, o = (dst & 1) * 8 + (n & 1) * 4;
    const f32x16& t = a[dst >> 1];
    const unsigned slot = ((q - dst - 1) & 3) * 2048;
    if (q != dst) lds_w128(base + ex + dst * 6144 + slot + (n & 1) * 1024, __builtin_bit_cast(u32x4, f32x4{t[o], t[o + 1], t[o + 2], t[o + 3]}));
  };
  f32x16 accb[2] = {}, accs[2] = {};
  u32x4 wf[2][2];
  wf[0][0] = read_w2(0, 0); wf[0][1] = read_w2(0, 1);
  wf[1][0] = read_w2(1, 0); wf[1][1] = read_w2(1, 1);
  SB();
  static_for<0, 36>([&](auto ic) {
    constexpr int J = decltype(ic)::value;
    f32x16 (&acc)[2] = J < 28 ? accb : accs;
    wait_for<vm_frag(J)>(af[J % RING]);
    if constexpr (J > 28) { pin(acc[0]); pin(acc[1]); }
    SB();
    acc[0] = mfma32(wf[J & 1][0], af[J % RING], acc[0]);
    SB();
    if constexpr (p2_barrier(J + 1)) {
      if constexpr (J + 1 == 17) wait_vm<ops_between(12, J)>();
      if constexpr (J + 1 == 27) wait_vm<ops_between(20, J)>();
      wg_barrier();
      SB();
    }
    if constexpr (J + 2 < 36) wf[J & 1][0] = read_w2(J + 2, 0);
    SB();
    acc[1] = mfma32(wf[J & 1][1], af[J % RING], acc[1]);
    SB();
    if constexpr (J + 2 < 36) wf[J & 1][1] = read_w2(J + 2, 1);
    if constexpr (J == J_FLAG) flag_load(c, c.fh_off, 16);
    if constexpr (J == J_CHECK) {
      wait_vm<n_frag(J_FLAG) + n_refill(J_FLAG) + ops_between(J_FLAG + 1, J)>();
      STAMP(11);
      flag_check(c, c.fh_off, 16);
      STAMP(12);
    }
    static_for<PA, 36>([&](auto fc) {
      if constexpr (frag_step(fc.value) == J) load16_sc1<0>(af[fc.value % RING], va, c.sws, sa + p2_ks(c, fc.value) * 1024);
    });
    if constexpr (n_refill(J)) issue_w2(c, J + 16);
    if constexpr (J >= 28) put(0, J - 28, accb);
    SB();
  });
  pin(xin); pin(accs[0]); pin(accs[1]);
  STAMP(13);
#pragma unroll
  for (int n = 0; n < 8; ++n) put(49152, n, accs);
  float b[8], sv[8];
#pragma unroll
  for (int e = 0; e < 8; ++e) {
    const float b0 = (q & 1) ? accb[0][8 + e] : accb[0][e], b1 = (q & 1) ? accb[1][8 + e] : accb[1][e];
    const float s0 = (q & 1) ? accs[0][8 + e] : accs[0][e], s1 = (q & 1) ? accs[1][8 + e] : accs[1][e];
    b[e] = (q & 2) ? b1 : b0;
    sv[e] = (q & 2) ? s1 : s0;
  }
  wg_barrier();
#pragma unroll
  for (int s = 0; s < 3; ++s) {
    const unsigned a = ex + q * 6144 + s * 2048;
    const f32x4 t0 = __builtin_bit_cast(f32x4, lds_r128(a)), t1 = __builtin_bit_cast(f32x4, lds_r128(a + 1024));
    const f32x4 t2 = __builtin_bit_cast(f32x4, lds_r128(a + 49152)), t3 = __builtin_bit_cast(f32x4, lds_r128(a + 50176));
#pragma unroll
    for (int e = 0; e < 4; ++e) { b[e] += t0[e]; b[4 + e] += t1[e]; sv[e] += t2[e]; sv[4 + e] += t3[e]; }
  }
  float y[8];
#pragma unroll
  for (int e = 0; e < 8; ++e) {
    const float sg = __builtin_amdgcn_rcpf(1.f + __builtin_amdgcn_exp2f(b[e] * -1.44269504088896f));
    y[e] = sv[e] * (b[e] * sg);
  }
#pragma unroll
  for (int e = 0; e < 4; ++e) {
    const auto t = __builtin_amdgcn_permlane32_swap(asu(y[e]), asu(y[4 + e]), false, false);
    y[e] = asf(t[0]);
    y[4 + e] = asf(t[1]);
  }
  u32x4 o;
#pragma unroll
  for (int e = 0; e < 4; ++e) o[e] = pack2(bf_lo(xin[e]) + y[2 * e], bf_hi(xin[e]) + y[2 * e + 1]);
  *(GAS u32x4*)(p.out + xo + c.col2) = o;
  STAMP(14);
}

__global__ __launch_bounds__(512, 2) void hh_fused_kernel(Args p) {
  extern __shared__ __attribute__((aligned(16))) char smem[];
  Ctx c;
  STAMP(0);
  c.lane = threadIdx.x & 63;
  c.w = __builtin_amdgcn_readfirstlane(threadIdx.x >> 6);
  const int xcd = blockIdx.x & 7, j = blockIdx.x >> 3;
  c.rt = 8 * (xcd & 1) + (j & 7);
  c.ct = 2 * (xcd >> 1) + (j >> 4);
  c.rt2 = 4 * xcd + (j & 3);
  c.col2 = (j >> 2) * 128;
  c.xrow = c.rt * 128 * LD_X;
  c.sx = make_srd(p.x, 2048 * LD_X);
  c.sws = make_srd(p.ws, WS_BYTES);
  c.sd = make_srd(p.wd, 1792 * LD_W2);
  c.so = make_srd(p.wo, 512 * LD_W2);
  c.sq = make_srd(p.wqkv, 512 * LD_QKV);
  c.sg = make_srd(p.wg, 512 * LD_GU);
  c.su = make_srd(p.wu, 512 * LD_GU);
  if (CODE_PF) {
    u64 pc;
    asm volatile("s_getpc_b64 %0" : "=s"(pc));
    const int line = j * 16 + (c.lane & 15);
    dma4(L_SINK, (c.w == 7 && c.lane < 16) ? line * 128 : OOB, make_srd((const char*)(pc & ~255ull), CODE_BYTES), 0);
  }
  if (PAGE_PF == 1) {
    const int i = j * 5 + c.lane;
    dma4(L_SINK, (c.w == 6 && c.lane < 5) ? i * 65536 : OOB, c.sws, 0);
    dma4(L_SINK, (c.w == 5 && c.lane < 1) ? j * 65536 : OOB, make_srd(p.out, 2048 * LD_X), 0);
  }
  if (PAGE_PF == 2) {
    dma4(L_SINK, (c.w == 6 && c.lane < 40) ? c.lane * 262144 : OOB, c.sws, 0);
    dma4(L_SINK, (c.w == 5 && c.lane < 8) ? c.lane * 262144 : OOB, make_srd(p.out, 2048 * LD_X), 0);
  }
  if (PAGE_PF == 3) {
    const int i = (j * 8 + c.w) * 10 + c.lane;
    dma4(L_SINK, c.lane < 10 ? i * 4096 : OOB, c.sws, 0);
    dma4(L_SINK, c.lane < 2 ? ((j * 8 + c.w) * 2 + c.lane) * 4096 : OOB, make_srd(p.out, 2048 * LD_X), 0);
  }
  if (PAGE_PF == 4) {
    dma4(L_SINK, (c.w == 5) ? (c.rt2 * 64 + c.lane) * LD_X : OOB, make_srd(p.out, 2048 * LD_X), c.col2);
  }
  c.fe_off = WS_FE + (c.rt2 >> 1) * 128;
  c.fh_off = WS_FH + (c.rt2 >> 1) * 128;
  asm volatile("" : "+s"(c.fe_off), "+s"(c.fh_off));
  c.rot1 = ROT1 ? 4 * ((j ^ (j >> 3) ^ (j >> 4)) & 1) : 0;
  c.rot2 = ROT2 ? ((j >> 2) + 2 * (j & 3)) & 7 : 0;
  if ((j >> 3) & 1) phase1<true>(p, c);
  else phase1<false>(p, c);
  phase2(p, c, ((j >> 3) & 1) * 8 + c.ct);
  STAMP_DUMP();
}

extern "C" void kernel_launch(void* const* d_in, const int* in_sizes, int n_in, void* d_out, int out_size,
                              void* d_ws, size_t ws_size, hipStream_t stream) {
  Args a;
  a.x = (const char*)d_in[0]; a.wqkv = (const char*)d_in[1]; a.wphi = (const char*)d_in[2]; a.wo = (const char*)d_in[3];
  a.wg = (const char*)d_in[5]; a.wu = (const char*)d_in[6]; a.wd = (const char*)d_in[7];
  a.ws = (char*)d_ws; a.out = (char*)d_out;
  (void)hipFuncSetAttribute((const void*)hh_fused_kernel, hipFuncAttributeMaxDynamicSharedMemorySize, LDS_BYTES);
  hh_fused_kernel<<<256, 512, LDS_BYTES, stream>>>(a);
}
```

```cpp
#define DSEL 0
#define DBLK 0
#include <hip/hip_runtime.h>
#include <hip/hip_bf16.h>
#include <stdint.h>

typedef unsigned long long u64;
typedef __attribute__((ext_vector_type(8))) __bf16 b16x8;
typedef __attribute__((ext_vector_type(2))) __bf16 b16x2;
typedef __attribute__((ext_vector_type(4))) short s16x4;
typedef __attribute__((ext_vector_type(2))) float f32x2;
typedef __attribute__((ext_vector_type(4))) float f32x4;
typedef __attribute__((ext_vector_type(16))) float f32x16;
typedef __attribute__((ext_vector_type(2))) unsigned u32x2;
typedef __attribute__((ext_vector_type(4))) unsigned u32x4;
typedef __attribute__((ext_vector_type(4))) int i32x4;
#define LAS __attribute__((address_space(3)))
#define GAS __attribute__((address_space(1)))
#define DI __device__ __forceinline__
#define SB() __builtin_amdgcn_sched_barrier(0)

constexpr int LD_X = 1024, LD_QKV = 3072, LD_GU = 3584, LD_W2 = 1024;
constexpr int NKS = 144;
constexpr unsigned WS_AFRAG = 0;
constexpr unsigned WS_KVP = 64u * NKS * 1024;
constexpr unsigned KVP_BYTES = 4608;
constexpr unsigned WS_STAT = WS_KVP + 8 * 16 * KVP_BYTES;
constexpr unsigned WS_FKV = WS_STAT + 2048 * 16;
constexpr unsigned WS_FE = WS_FKV + 8 * 128;
constexpr unsigned WS_FH = WS_FE + 16 * 128;
constexpr unsigned WS_ARR = WS_FH + 16 * 128;
constexpr unsigned WS_EPOCH = WS_ARR + 256 * 8;
constexpr unsigned WS_BYTES = WS_EPOCH + 128;
constexpr unsigned OOB = 0x7ffffff0u;
constexpr unsigned SPIN_CAP = 1u << 20;

constexpr int L_X = 0;
constexpr int L_PK = 0;
constexpr int L_V = 8 * 4352;
constexpr int L_PHI = 53248;
constexpr int L_CS = 55296;
constexpr int L_SINK = 59392;
constexpr int L_KVT = 60416;
constexpr int L_W = 65536;
constexpr int LDS_BYTES = 163840;
template <bool K> constexpr int l_stage(int g) {
  if (K) return g < 8 ? (g % 3) * 16384 : (g - 8) % 3 == 2 ? 24576 : 49152 + ((g - 8) % 3) * 24576;
  return g < 8 ? ((g + 2) % 3) * 32768 : 32768 + (g - 8) * 8192;
}
constexpr int l_step(int j) { return (j % 18 < 6 ? j % 18 : j % 18 + 2) * 8192; }

#define STAMP(n) do { if ((n) == 0 || (n) == DSEL) { SB(); u64 t_; asm volatile("s_memrealtime %0\n\ts_waitcnt lgkmcnt(0)" : "=s"(t_) :: "memory"); if ((n) == 0) c.d0 = t_; else c.d1 = t_; SB(); } } while (0)
#define STAMP_DUMP() do { if (blockIdx.x == DBLK && DSEL > 0) { const u64 e_ = __builtin_amdgcn_s_memrealtime() + (c.d1 - c.d0); while (__builtin_amdgcn_s_memrealtime() < e_) __builtin_amdgcn_s_sleep(2); } } while (0)

#define CODE_PF 1
#define CODE_BYTES 46000
#define ROT1 0
#define ROT2 1
struct Args {
  const char* x; const char* wqkv; const char* wphi; const char* wo;
  const char* wg; const char* wu; const char* wd;
  char* ws; char* out;
};

DI i32x4 make_srd(const char* p, unsigned bytes) {
  const u64 a = (u64)p;
  i32x4 r;
  r[0] = __builtin_amdgcn_readfirstlane((int)(unsigned)a);
  r[1] = __builtin_amdgcn_readfirstlane((int)(unsigned)(a >> 32) & 0xffff);
  r[2] = (int)bytes;
  r[3] = 0x00020000;
  return r;
}
DI i32x4 sel_srd(bool first, i32x4 a, i32x4 b) {
  i32x4 r;
  const int a0 = a[0], a1 = a[1], a2 = a[2], b0 = b[0], b1 = b[1], b2 = b[2];
  r[0] = first ? a0 : b0; r[1] = first ? a1 : b1; r[2] = first ? a2 : b2; r[3] = 0x00020000;
  return r;
}
DI void dma16(unsigned lds, unsigned voff, i32x4 srd, unsigned soff) {
  asm volatile("s_mov_b32 m0, %0\n\ts_nop 0\n\tbuffer_load_dwordx4 %1, %2, %3 offen lds"
               :: "s"(lds), "v"(voff), "s"(srd), "s"(soff) : "memory");
}
DI void dma4(unsigned lds, unsigned voff, i32x4 srd, unsigned soff) {
  asm volatile("s_mov_b32 m0, %0\n\ts_nop 0\n\tbuffer_load_dword %1, %2, %3 offen lds"
               :: "s"(lds), "v"(voff), "s"(srd), "s"(soff) : "memory");
}
DI void dma16g(unsigned lds, const char* gptr) {
  asm volatile("s_mov_b32 m0, %0\n\ts_nop 0\n\tglobal_load_lds_dwordx4 %1, off" :: "s"(lds), "v"(gptr) : "memory");
}
template <int IMM> DI void load16_sc1(u32x4& d, unsigned voff, i32x4 srd, unsigned soff) {
  asm volatile("buffer_load_dwordx4 %0, %1, %2, %3 offen offset:%4 sc1" : "=v"(d) : "v"(voff), "s"(srd), "s"(soff), "n"(IMM) : "memory");
}
DI void load16(u32x4& d, unsigned voff, i32x4 srd, unsigned soff) {
  asm volatile("buffer_load_dwordx4 %0, %1, %2, %3 offen" : "=v"(d) : "v"(voff), "s"(srd), "s"(soff) : "memory");
}
DI void load8_sc1(u32x2& d, unsigned voff, i32x4 srd, unsigned soff) {
  asm volatile("buffer_load_dwordx2 %0, %1, %2, %3 offen sc1" : "=v"(d) : "v"(voff), "s"(srd), "s"(soff) : "memory");
}
DI void store16_sc1(u32x4 v, unsigned voff, i32x4 srd, unsigned soff) {
  asm volatile("buffer_store_dwordx4 %0, %1, %2, %3 offen sc1\n\ts_nop 1" :: "v"(v), "v"(voff), "s"(srd), "s"(soff) : "memory");
}
DI void store8_sc1(u32x2 v, unsigned voff, i32x4 srd, unsigned soff) {
  asm volatile("buffer_store_dwordx2 %0, %1, %2, %3 offen sc1\n\ts_nop 1" :: "v"(v), "v"(voff), "s"(srd), "s"(soff) : "memory");
}
template <int N> DI void wait_vm() { asm volatile("s_waitcnt vmcnt(%0)" :: "n"(N) : "memory"); }
template <int N, class T> DI void wait_for(T& v) { asm volatile("s_waitcnt vmcnt(%1)" : "+v"(v) : "n"(N) : "memory"); }
template <class T> DI void pin(T& v) { asm volatile("" : "+v"(v)); }
DI void wg_barrier() {
  asm volatile("s_waitcnt lgkmcnt(0)" ::: "memory");
  __builtin_amdgcn_s_barrier();
  asm volatile("" ::: "memory");
}
DI u32x4 lds_r128(unsigned a) { return *(const LAS u32x4*)a; }
DI void lds_w128(unsigned a, u32x4 v) { *(LAS u32x4*)a = v; }
DI u32x4 lds_tr2(unsigned a0, unsigned a1) {
  const s16x4 lo = __builtin_amdgcn_ds_read_tr16_b64_v4i16((LAS s16x4*)a0);
  const s16x4 hi = __builtin_amdgcn_ds_read_tr16_b64_v4i16((LAS s16x4*)a1);
  const u32x2 l = __builtin_bit_cast(u32x2, lo), h = __builtin_bit_cast(u32x2, hi);
  return u32x4{l[0], l[1], h[0], h[1]};
}
DI f32x16 mfma32(u32x4 a, u32x4 b, f32x16 c) {
  return __builtin_amdgcn_mfma_f32_32x32x16_bf16(__builtin_bit_cast(b16x8, a), __builtin_bit_cast(b16x8, b), c, 0, 0, 0);
}
DI f32x4 mfma16(u32x4 a, u32x4 b, f32x4 c) {
  return __builtin_amdgcn_mfma_f32_16x16x32_bf16(__builtin_bit_cast(b16x8, a), __builtin_bit_cast(b16x8, b), c, 0, 0, 0);
}
DI unsigned pack2(float lo, float hi) {
  const b16x2 v = __builtin_convertvector(f32x2{lo, hi}, b16x2);
  return __builtin_bit_cast(unsigned, v);
}
DI float asf(unsigned v) { return __builtin_bit_cast(float, v); }
DI unsigned asu(float v) { return __builtin_bit_cast(unsigned, v); }
DI float bf_lo(unsigned v) { return asf(v << 16); }
DI float bf_hi(unsigned v) { return asf(v & 0xffff0000u); }
DI float half_sum(float v) {
  const auto r = __builtin_amdgcn_permlane32_swap(asu(v), asu(v), false, false);
  return asf(r[0]) + asf(r[1]);
}
DI float half_max(float v) {
  const auto r = __builtin_amdgcn_permlane32_swap(asu(v), asu(v), false, false);
  return fmaxf(asf(r[0]), asf(r[1]));
}
template <int CTRL> DI float dpp(float v) {
  return asf((unsigned)__builtin_amdgcn_update_dpp(0, (int)asu(v), CTRL, 0xf, 0xf, true));
}
DI float row_sum(float s) {
  s += dpp<0xB1>(s); s += dpp<0x4E>(s); s += dpp<0x141>(s); s += dpp<0x140>(s);
  return s;
}
DI float wave_sum(float s) {
  s = row_sum(s);
  const auto r = __builtin_amdgcn_permlane16_swap(asu(s), asu(s), false, false);
  return half_sum(asf(r[0]) + asf(r[1]));
}
DI u32x4 pack8(const f32x16& a, int o, float s) {
  return u32x4{pack2(a[o] * s, a[o + 1] * s), pack2(a[o + 2] * s, a[o + 3] * s),
               pack2(a[o + 4] * s, a[o + 5] * s), pack2(a[o + 6] * s, a[o + 7] * s)};
}
constexpr unsigned ONES2 = 0x3f803f80u;
template <int V> struct IC { static constexpr int value = V; };
template <int I, int N, class F> DI void static_for(F&& f) {
  if constexpr (I < N) { f(IC<I>{}); static_for<I + 1, N>(f); }
}

DI float max3(float a, float b, float c) { float r; asm("v_max3_f32 %0, %1, %2, %3" : "=v"(r) : "v"(a), "v"(b), "v"(c)); return r; }
DI float nmax3(float a, float b, float c) { float r; asm("v_max3_f32 %0, %1, -%2, -%3" : "=v"(r) : "v"(a), "v"(b), "v"(c)); return r; }
DI float max2(float a, float b) { float r; asm("v_max_f32 %0, %1, %2" : "=v"(r) : "v"(a), "v"(b)); return r; }
template <class F> DI float fold2(float a, float b, F op) {
  const auto r = __builtin_amdgcn_permlane32_swap(asu(a), asu(b), false, false);
  return op(asf(r[0]), asf(r[1]));
}
DI void spread2(float v, float& a, float& b) {
  const auto r = __builtin_amdgcn_permlane32_swap(asu(v), asu(v), false, false);
  a = asf(r[0]); b = asf(r[1]);
}
DI void dual_exp(const f32x16& p, int o, f32x2 (&ep)[4], f32x2 (&en)[4], float& sp, float& sn) {
  const float L2E = 1.44269504088896f;
  const float mx = max2(max3(max3(max3(p[o], p[o + 1], p[o + 2]), p[o + 3], p[o + 4]), p[o + 5], p[o + 6]), p[o + 7]);
  const float nm = nmax3(nmax3(nmax3(nmax3(-3.0e38f, p[o], p[o + 1]), p[o + 2], p[o + 3]), p[o + 4], p[o + 5]), p[o + 6], p[o + 7]);
  float a, b;
  spread2(fold2(mx, nm, max2) * -L2E, a, b);
  const f32x2 l2 = {L2E, L2E}, a2 = {a, a}, b2 = {b, b};
#pragma unroll
  for (int i = 0; i < 4; ++i) {
    const f32x2 v = {p[o + 2 * i], p[o + 2 * i + 1]};
    const f32x2 tp = __builtin_elementwise_fma(v, l2, a2), tn = __builtin_elementwise_fma(-v, l2, b2);
    ep[i] = f32x2{__builtin_amdgcn_exp2f(tp[0]), __builtin_amdgcn_exp2f(tp[1])};
    en[i] = f32x2{__builtin_amdgcn_exp2f(tn[0]), __builtin_amdgcn_exp2f(tn[1])};
  }
  const f32x2 s2 = (ep[0] + ep[1]) + (ep[2] + ep[3]), t2 = (en[0] + en[1]) + (en[2] + en[3]);
  sp = s2[0] + s2[1]; sn = t2[0] + t2[1];
}
DI u32x4 pack8s(const f32x2 (&e)[4], float s) {
  u32x4 r;
#pragma unroll
  for (int i = 0; i < 4; ++i) { const f32x2 t = e[i] * s; r[i] = pack2(t[0], t[1]); }
  return r;
}
DI void phi_k(const f32x16& p, int o, u32x4& pos, u32x4& neg) {
  f32x2 ep[4], en[4];
  float sp, sn, rp, rn;
  dual_exp(p, o, ep, en, sp, sn);
  spread2(__builtin_amdgcn_rcpf(fold2(sp, sn, [](float x, float y) { return x + y; })), rp, rn);
  pos = pack8s(ep, rp);
  neg = pack8s(en, rn);
}
DI void phi_q(const f32x16& p, int o, u32x4& pos, u32x4& neg, float& eps) {
  f32x2 ep[4], en[4];
  float sp, sn;
  dual_exp(p, o, ep, en, sp, sn);
  spread2(fold2(sp, sn, [](float x, float y) { return x + y; }), sp, sn);
#pragma unroll
  for (int i = 0; i < 4; ++i) pos[i] = pack2(ep[i][0], ep[i][1]);
  neg = pack8s(en, sp * __builtin_amdgcn_rcpf(sn));
  eps = sp * 1e-6f;
}

enum Op { OP_NONE, OP_X0, OP_X1, OP_W0, OP_W1, OP_W2, OP_W3, OP_TOUCH0, OP_TOUCH1, OP_TOUCH2, OP_STAT, OP_P2W, OP_FE };
struct Slot { int op, arg; };
constexpr int NEVER = 99, N_SLOT = 7;
template <bool K> constexpr Slot slot_at(int t, int n) {
  constexpr int NU = K ? 2 : 4;
  if (t < 8) {
    if (n == 0) return t == 0 ? Slot{OP_TOUCH0, 0} : t == 2 ? Slot{OP_TOUCH1, 0} : t == 4 ? Slot{OP_TOUCH2, 0} : t == 6 ? Slot{OP_STAT, 0} : Slot{OP_NONE, 0};
    if (t < 6) return n < 3 ? Slot{OP_X0 + n - 1, t + 2} : n - 3 < NU ? Slot{OP_W0 + n - 3, t + 2} : Slot{OP_NONE, 0};
    if (K) return n < 4 ? Slot{OP_W0 + n - 1, t + 2} : Slot{OP_NONE, 0};
    return n < 5 ? Slot{OP_W0, 8 + 4 * (t - 6) + n - 1} : Slot{OP_NONE, 0};
  }
  if (!K || t == 8) return Slot{OP_NONE, 0};
  if (t <= 13) return n < 3 ? Slot{OP_W0 + n, t + 2} : n == 3 ? Slot{OP_P2W, t - 9} : (n == 4 && t == 13) ? Slot{OP_P2W, 5} : Slot{OP_NONE, 0};
  if (n < 3) return Slot{OP_P2W, 9 + 3 * (t - 14) + n};
  return (n == 3 && t == 15) ? Slot{OP_FE, 0} : Slot{OP_NONE, 0};
}
template <bool K> constexpr int op_needed_by(Slot s) {
  if (s.op >= OP_X0 && s.op <= OP_W3) return (!K && s.arg > 8) ? 8 : s.arg;
  return s.op == OP_STAT ? 8 : NEVER;
}
template <bool K> constexpr int vm_at(int g) {
  if (g == 0) return K ? 4 : 6;
  if (g == 9) return 8;
  int n = 0;
  for (int t = g - 1; t >= 0; --t) {
    if (t == 8 && g >= 10) return n;
    for (int i = N_SLOT - 1; i >= 0; --i) {
      const Slot s = slot_at<K>(t, i);
      if (s.op != OP_NONE && op_needed_by<K>(s) <= g) return n;
      n += s.op != OP_NONE;
    }
  }
  return n;
}

struct Ctx {
  i32x4 sx, sq, sg, su, sws, sd, so;
  unsigned vx0, vx1, vwq, vwg, vw2;
  const char* vmix;
  unsigned xrow;
  unsigned col2;
  int w, lane, rt, ct, rt2;
  unsigned fe_off, fh_off;
  int rot1, rot2;
  unsigned xa[4];
  unsigned wa[2][4];
  u32x2 epoch, flag;
  u32x4 srow, stat;
  u64 d0, d1;
};

DI int k_slice(const Ctx& c, int g) { return (g + c.rot1) & 7; }
DI void issue_x(const Ctx& c, int j, int n) {
  dma16(L_X + (j % 3) * 16384 + c.w * 2048 + n * 1024, n ? c.vx1 + 0u : c.vx0 + 0u, c.sx, c.xrow + 128 * k_slice(c, j));
}
template <bool K> DI void issue_w(const Ctx& c, int g, int u) {
  const unsigned l = L_W + l_stage<K>(g) + u * 8192 + c.w * 1024;
  const unsigned k0 = k_slice(c, g) * 64;
  if (K ? g < 8 : g >= 8) dma16(l, c.vwq, c.sq, c.ct * 128 + k0 * LD_QKV + (K ? 1024 + u * 1024 : 0));
  else if (K && u == 2) dma16g(l, c.vmix + k0 * LD_GU);
  else dma16(l, c.vwg, sel_srd(u & 1, c.su, c.sg), k0 * LD_GU + (K ? 2048 + c.ct * 128 : c.ct * 256 + (u >> 1) * 128));
}
DI int p2_ks(const Ctx& c, int j) {
  const int q = c.w & 3;
  if (j < 16) return 16 * q + ((j + 2 * c.rot2) & 15);
  if (j >= 28) return 112 + 8 * q + ((j - 28 + c.rot2) & 7);
  const int t = j - 16 + ((3 * c.rot2) >> 1);
  return 64 + 12 * q + (t >= 12 ? t - 12 : t);
}
DI void issue_w2(const Ctx& c, int j) {
  const int q = c.w & 3, m = c.w >> 2;
  dma16(l_step(j) + q * 2048 + m * 1024, c.vw2, sel_srd(j >= 28, c.so, c.sd),
        c.col2 + ((p2_ks(c, j) - (j >= 28 ? 112 : 0)) * 16 + m * 8) * LD_W2);
}
template <bool K> DI void touch(const Ctx& c, int which) {
  const unsigned sink = L_SINK + (c.w & 3) * 256;
  const int odd = c.w & 1, hw = c.w >> 1, l = c.lane;
  if (which == 2) {
    const bool wo = c.w == 7;
    const int row = wo ? 16 * c.rt2 + l : 56 * c.rt2 + 8 * c.w + l;
    dma4(sink, l < (wo ? 16 : 8) ? row * LD_W2 : OOB, sel_srd(wo, c.so, c.sd), c.col2);
  } else if (K == (which == 0)) {
    const int row = 32 * c.rt + hw * 8 + l;
    dma4(sink, (l < 8 && (K || !odd)) ? row * LD_QKV : OOB, c.sq, c.ct * 128 + (K ? 1024 + odd * 1024 : 0));
  } else {
    const int row = 32 * c.rt + hw * 8 + (l >> 1);
    const unsigned cb = K ? ((l & 1) ? 3072 + 64 * c.ct : 2048 + 128 * c.ct) : 256 * c.ct + (l & 1) * 128;
    dma4(sink, l < 16 ? row * LD_GU + cb : OOB, sel_srd(odd, c.su, c.sg), 0);
  }
}
DI void flag_load(Ctx& c, unsigned off, int n) { load8_sc1(c.flag, (c.lane & (n - 1)) * 8, c.sws, off); }
DI void flag_check(Ctx& c, unsigned off, int n) {
  pin(c.flag);
  for (unsigned spin = 0; spin < SPIN_CAP && !__all(c.flag[0] == c.epoch[0] && c.flag[1] == c.epoch[1]); ++spin) {
    flag_load(c, off, n);
    wait_for<0>(c.flag);
  }
}
DI void flag_store(const Ctx& c, unsigned off) {
  if (c.w == 0) store8_sc1(c.epoch, c.lane == 0 ? 0 : OOB, c.sws, off);
}
template <bool K, int T, int N> DI void run_slot(Ctx& c) {
  constexpr Slot s = slot_at<K>(T, N);
  if constexpr (s.op == OP_X0 || s.op == OP_X1) issue_x(c, s.arg, s.op - OP_X0);
  if constexpr (s.op >= OP_W0 && s.op <= OP_W3) issue_w<K>(c, s.arg, s.op - OP_W0);
  if constexpr (s.op >= OP_TOUCH0 && s.op <= OP_TOUCH2) touch<K>(c, s.op - OP_TOUCH0);
  if constexpr (s.op == OP_STAT) load16_sc1<0>(c.stat, (32 * (c.w & 3) + (c.lane & 31)) * 16, c.sws, WS_STAT + c.rt * 2048);
  if constexpr (s.op == OP_P2W) issue_w2(c, s.arg);
  if constexpr (s.op == OP_FE) flag_load(c, c.fe_off, 8);
}
template <bool K, int G> DI void open_stage(Ctx& c) {
  wait_vm<vm_at<K>(G)>(); wg_barrier();
  if (K && G == 10) flag_store(c, WS_FKV + c.ct * 128 + c.rt * 8);
}
template <bool K> DI u32x4 read_w(const Ctx& c, int g, int i, int u) {
  const int o = l_stage<K>(g) + u * 8192;
  const unsigned a = c.wa[o >= 49152][i] + o % 49152;
  return lds_tr2(a, a + 512);
}

struct RowStat { float v[8]; float s; float mean; };
DI void stat_step(Ctx& c, RowStat& t, int n, int c16) {
  if (n == 0) {
    pin(c.srow); pin(c.epoch);
#pragma unroll
    for (int e = 0; e < 4; ++e) { t.v[2 * e] = bf_lo(c.srow[e]); t.v[2 * e + 1] = bf_hi(c.srow[e]); pin(t.v[2 * e]); pin(t.v[2 * e + 1]); }
  }
  if (n == 1) { t.s = ((t.v[0] + t.v[1]) + (t.v[2] + t.v[3])) + ((t.v[4] + t.v[5]) + (t.v[6] + t.v[7])); pin(t.s); }
  if (n == 2) { t.s = row_sum(t.s); pin(t.s); }
  if (n == 3) {
    const auto r = __builtin_amdgcn_permlane16_swap(asu(t.s), asu(t.s), false, false);
    t.mean = half_sum(asf(r[0]) + asf(r[1])) * (1.f / 512.f); pin(t.mean);
  }
  if (n == 4) {
#pragma unroll
    for (int e = 0; e < 8; ++e) { t.v[e] -= t.mean; pin(t.v[e]); }
  }
  if (n == 5) {
    float a = t.v[0] * t.v[0], b = t.v[1] * t.v[1];
#pragma unroll
    for (int e = 2; e < 8; e += 2) { a = fmaf(t.v[e], t.v[e], a); b = fmaf(t.v[e + 1], t.v[e + 1], b); }
    t.s = a + b; pin(t.s);
  }
  if (n == 6) { t.s = row_sum(t.s); pin(t.s); }
  if (n == 7) {
    const auto r = __builtin_amdgcn_permlane16_swap(asu(t.s), asu(t.s), false, false);
    t.s = __builtin_amdgcn_rsqf(half_sum(asf(r[0]) + asf(r[1])) * (1.f / 512.f) + 1e-5f); pin(t.s);
  }
  if (n == 8) {
    const u64 e = (((u64)c.epoch[1] << 32) | c.epoch[0]) + 1;
    c.epoch = u32x2{(unsigned)e, (unsigned)(e >> 32)};
    store16_sc1(u32x4{asu(t.mean), c.epoch[0], asu(t.s), c.epoch[0]}, c.lane == 0 ? 0 : OOB, c.sws,
                WS_STAT + (c.rt * 128 + c16 * 8 + c.w) * 16);
    if (c.w == 0) store8_sc1(c.epoch, c.lane == 0 ? 0 : OOB, c.sws, WS_ARR + blockIdx.x * 8);
  }
}

template <bool K, int NU, int G0, int PD, class H>
DI void gemm_pass(Ctx& c, u32x4 (&xf)[32], f32x16 (&acc)[NU], float (&cs)[NU], int c16, H&& hook) {
  u32x4 wf[PD][NU];
  RowStat rs;
  if (G0 == 0) xf[0] = lds_r128(c.xa[0]);
#pragma unroll
  for (int u = 0; u < NU; ++u) {
#pragma unroll
    for (int d = 0; d < PD; ++d) wf[d][u] = read_w<K>(c, G0, d, u);
    cs[u] = 0.f;
#pragma unroll
    for (int e = 0; e < 16; ++e) acc[u][e] = 0.f;
  }
  SB();
  static_for<0, 32 * NU>([&](auto ic) {
    constexpr int idx = decltype(ic)::value / NU, u = decltype(ic)::value % NU;
    constexpr int g = G0 + idx / 4, i = idx & 3, gn = G0 + (idx + PD) / 4, in = (idx + PD) & 3;
    constexpr bool staged = K || G0 == 0, opens = i == 3 && g < (K ? 15 : 7);
    u32x4& f = wf[idx % PD][u];
    cs[u] += bf_lo(f[i]);
    cs[u] += bf_hi(f[i]);
    pin(cs[u]);
    SB();
    acc[u] = mfma32(f, xf[idx], acc[u]);
    SB();
    if constexpr (u == 0 && opens) { open_stage<K, g + 1>(c); SB(); }
    if constexpr (u == 0 && G0 == 0 && idx + 1 < 32) xf[idx + 1] = lds_r128(c.xa[(idx + 1) & 3] + (((idx + 1) / 4) % 3) * 16384);
    if constexpr (idx + PD < 32) f = read_w<K>(c, gn, in, u);
    if constexpr (G0 == 0 && u < 2 && 2 * idx + u < 9) stat_step(c, rs, 2 * idx + u, c16);
    if constexpr (staged) {
      constexpr int t = i == 3 ? g + 1 : g, pos = i == 3 ? u : NU * (i + 1) + u;
      constexpr int n = t == 0 ? pos - NU : NU == 2 ? pos - 1 : (pos & 1) ? pos / 2 : -1;
      if constexpr (t < 16 && n >= 0 && n < N_SLOT) run_slot<K, t, n>(c);
    }
    if constexpr (u == NU - 1) hook(IC<idx>{});
    SB();
  });
#pragma unroll
  for (int u = 0; u < NU; ++u) pin(acc[u]);
}

template <int NU> DI void put_colsum(const float (&cs)[NU], int wm, int wn, int r) {
#pragma unroll
  for (int u = 0; u < NU; ++u) {
    const float t = half_sum(cs[u]);
    const float hi = bf_lo(pack2(t, 0.f));
    *(LAS unsigned*)(L_CS + ((u * 2 + wn) * 32 + r) * 16 + wm * 4) = pack2(hi, t - hi);
  }
}
template <int NU> DI void apply_mean(f32x16 (&acc)[NU], u32x4 negm, int wn, int r) {
#pragma unroll
  for (int u = 0; u < NU; ++u) acc[u] = mfma32(lds_r128(L_CS + ((u * 2 + wn) * 32 + r) * 16), negm, acc[u]);
}
DI void store_frag(const Ctx& c, int tb, int ks, u32x4 v) {
  store16_sc1(v, c.lane * 16, c.sws, WS_AFRAG + (tb * NKS + ks) * 1024);
}
DI void store_h(const Ctx& c, int tb, int ks, const f32x16& g, const f32x16& u, float r2) {
  f32x16 h;
#pragma unroll
  for (int e = 0; e < 16; ++e) h[e] = g[e] * u[e];
  store_frag(c, tb, ks, pack8(h, 0, r2));
  store_frag(c, tb, ks + 1, pack8(h, 8, r2));
}

template <bool K>
DI void phase1(const Args& p, Ctx& c) {
  constexpr int NU1 = K ? 2 : 4, NU2 = K ? 3 : 1;
  const int tid = threadIdx.x, lane = c.lane, w = c.w, rt = c.rt, ct = c.ct;
  const int wm = w & 3, wn = w >> 2, r = lane & 31, hh = lane >> 5;
  const int blk = (lane >> 4) & 1, q4 = (lane & 15) >> 2, p4 = lane & 3;
  const int tb = rt * 4 + wm, c16 = (K ? 8 : 0) + ct;
  {
    const int row = 16 * w + (lane >> 3), ch = (lane & 7) ^ ((row >> 1) & 7);
    c.vx0 = row * LD_X + ch * 16;
    c.vx1 = (row + 8) * LD_X + (ch ^ 4) * 16;
    issue_x(c, 0, 0); issue_x(c, 0, 1);
    const int wrow = 8 * w + (lane >> 3), wch = (lane & 7) ^ (((lane >> 4) & 1) << 2);
    c.vw2 = (lane >> 3) * LD_W2 + wch * 16;
    c.vwq = wrow * LD_QKV + wch * 16;
    c.vwg = wrow * LD_GU + wch * 16;
    c.vmix = (((wch >> 1) & 1) ? p.wu : p.wg) + wrow * LD_GU + (1536 + 32 * ct + 16 * (wch >> 2) + 8 * (wch & 1)) * 2;
  }
#pragma unroll
  for (int u = 0; u < NU1; ++u) issue_w<K>(c, 0, u);
  load8_sc1(c.epoch, 0, c.sws, WS_EPOCH);
  load16(c.srow, lane * 16, c.sx, c.xrow + (c16 * 8 + w) * LD_X);
  dma4(L_PHI + w * 256, tid * 4, make_srd(p.wphi, 32 * 512), ct * 2048);
#pragma unroll
  for (int i = 0; i < 4; ++i) {
    const int s = (i + wm) & 3;
    c.xa[i] = L_X + (32 * wm + r) * 128 + (((2 * s + hh) ^ ((r >> 1) & 7)) * 16);
    c.wa[0][i] = L_W + 2048 * s + (8 * hh + q4) * 128 + ((wn ^ (q4 >> 1)) * 64) + 32 * blk + 8 * p4;
    c.wa[1][i] = c.wa[0][i] + 49152;
    pin(c.xa[i]); pin(c.wa[0][i]); pin(c.wa[1][i]);
  }
  __builtin_amdgcn_s_barrier();
  issue_x(c, 1, 0); issue_x(c, 1, 1);
#pragma unroll
  for (int u = 0; u < NU1; ++u) issue_w<K>(c, 1, u);
  open_stage<K, 0>(c);
  STAMP(1);

  u32x4 xf[32];
  f32x16 acc[NU1], a2[NU2];
  float cs[NU1], cs2[NU2];
  gemm_pass<K, NU1, 0, 1>(c, xf, acc, cs, c16, [](auto) {});
  STAMP(2);

  put_colsum<NU1>(cs, wm, wn, r);
  if (!K) wait_vm<8>();
  pin(c.stat);
  for (unsigned spin = 0; spin < SPIN_CAP && !__all(c.stat[1] == c.epoch[0] && c.stat[3] == c.epoch[0]); ++spin) {
    load16_sc1<0>(c.stat, (32 * wm + r) * 16, c.sws, WS_STAT + rt * 2048);
    wait_for<0>(c.stat);
  }
  const float rstd = asf(c.stat[2]), r2 = rstd * rstd;
  u32x4 negm;
  {
    const float nm = -asf(c.stat[0]);
    const float mhi = bf_lo(pack2(nm, 0.f));
    const float mm = hh ? (nm - mhi) : mhi;
    const unsigned m2 = pack2(mm, mm);
    negm = u32x4{m2, m2, m2, m2};
  }
  const f32x16 zero16 = {};
  auto logits = [&](const f32x16& a) {
    const unsigned pa = L_PHI + (2 * wn + blk) * 512 + (4 * hh + q4) * 32 + 8 * p4;
    const u32x4 wp = lds_tr2(pa, pa + 256), z = {0u, 0u, 0u, 0u};
    const f32x16 t = mfma32(blk ? z : wp, pack8(a, 0, rstd), zero16);
    return mfma32(blk ? wp : z, pack8(a, 8, rstd), t);
  };
  wg_barrier();
  apply_mean<NU1>(acc, negm, wn, r);

  if constexpr (K) {
    const unsigned img = hh * 2176 + (32 * wm + r) * 16;
    issue_w<K>(c, 10, 0);
    const f32x16 pl = logits(acc[0]);
    issue_w<K>(c, 10, 1);
#pragma unroll
    for (int s = 0; s < 2; ++s) lds_w128(L_V + (2 * wn + s) * 4352 + img, pack8(acc[1], 8 * s, rstd));
    issue_w<K>(c, 10, 2);
#pragma unroll
    for (int s = 0; s < 2; ++s) {
      u32x4 pos, neg;
      phi_k(pl, 8 * s, pos, neg);
      lds_w128(L_PK + (2 * wn + s) * 4352 + img, pos);
      lds_w128(L_PK + (4 + 2 * wn + s) * 4352 + img, neg);
      issue_w2(c, 6 + s);
    }
    issue_w2(c, 8);
    wg_barrier();
    STAMP(3);
    const unsigned kvp = WS_KVP + (ct * 16 + rt) * KVP_BYTES;
    const unsigned t = (p4 >> 1) * 2176 + (4 * (lane >> 4) + q4) * 16 + (p4 & 1) * 8;
    const u32x4 ones = {ONES2, ONES2, ONES2, ONES2};
    f32x4 kv = {}, z = {};
#pragma unroll
    for (int ks = 0; ks < 4; ++ks) {
      const unsigned ap = L_PK + w * 4352 + t + ks * 512, av = L_V + (w & 3) * 4352 + t + ks * 512;
      const u32x4 fa = lds_tr2(ap, ap + 256), fb = lds_tr2(av, av + 256);
      kv = mfma16(fa, fb, kv);
      z = mfma16(fa, ones, z);
    }
    store8_sc1(u32x2{pack2(kv[0], kv[1]), pack2(kv[2], kv[3])}, tid * 8, c.sws, kvp);
    asm volatile("s_nop 7" : "+v"(z));
    store16_sc1(__builtin_bit_cast(u32x4, z), (lane & 15) == 0 ? (lane >> 4) * 16 : OOB, c.sws, kvp + 4096 + w * 64);
    STAMP(4);
    gemm_pass<K, NU2, 8, 1>(c, xf, a2, cs2, c16, [](auto) {});
    STAMP(6);
    put_colsum<NU2>(cs2, wm, wn, r);
    wg_barrier();
    STAMP(8);
    apply_mean<NU2>(a2, negm, wn, r);
    issue_w2(c, 15); issue_w2(c, 16); issue_w2(c, 17);
    store_h(c, tb, 64 + 4 * ct + 2 * wn, a2[0], a2[1], r2);
    f32x16 h;
#pragma unroll
    for (int e = 0; e < 8; ++e) h[e] = a2[2][e] * a2[2][8 + e];
    store_frag(c, tb, 96 + 2 * ct + wn, pack8(h, 0, r2));
    wait_vm<6>();
  } else {
    issue_w2(c, 0);
    store_h(c, tb, 8 * ct + 2 * wn, acc[0], acc[1], r2);
    issue_w2(c, 1);
    store_h(c, tb, 8 * ct + 4 + 2 * wn, acc[2], acc[3], r2);
    wait_vm<6>();
    wg_barrier();
    STAMP(4);
    gemm_pass<K, NU2, 8, 3>(c, xf, a2, cs2, c16, [&](auto ic) {
      constexpr int idx = decltype(ic)::value;
      if constexpr (idx < 16 && (idx & 1)) issue_w2(c, 2 + idx / 2);
      if constexpr (idx == 20) flag_load(c, WS_FKV + ct * 128, 16);
    });
    STAMP(6);
    put_colsum<NU2>(cs2, wm, wn, r);
    wait_vm<0>();
    flag_check(c, WS_FKV + ct * 128, 16);
    const unsigned kvp = WS_KVP + ct * 16 * KVP_BYTES;
    u32x4 part[8], zpart;
    wg_barrier();
    flag_store(c, WS_FE + rt * 128 + ct * 8);
    apply_mean<NU2>(a2, negm, wn, r);
#pragma unroll
    for (int i = 0; i < 5; ++i) load16_sc1<0>(part[i], (lane & 1) * KVP_BYTES + (tid >> 1) * 16, c.sws, kvp + 2 * i * KVP_BYTES);
    const f32x16 pl = logits(a2[0]);
#pragma unroll
    for (int i = 5; i < 8; ++i) load16_sc1<0>(part[i], (lane & 1) * KVP_BYTES + (tid >> 1) * 16, c.sws, kvp + 2 * i * KVP_BYTES);
    load16_sc1<0>(zpart, (lane & 15) * KVP_BYTES + (lane >> 4) * 16, c.sws, kvp + 4096 + w * 64);
    u32x4 fq[2][2];
    float eps[2];
    phi_q(pl, 0, fq[0][0], fq[0][1], eps[0]);
    pin(fq[0][0]); pin(fq[0][1]);
    issue_w2(c, 10); issue_w2(c, 11); issue_w2(c, 12); issue_w2(c, 13);
    phi_q(pl, 8, fq[1][0], fq[1][1], eps[1]);
    pin(fq[1][0]); pin(fq[1][1]);
    issue_w2(c, 14); issue_w2(c, 15); issue_w2(c, 16); issue_w2(c, 17);
    flag_load(c, c.fe_off, 8);
    wait_vm<9>();
    {
#pragma unroll
      for (int i = 0; i < 8; ++i) pin(part[i]);
      pin(zpart);
      float v[8] = {};
#pragma unroll
      for (int i = 0; i < 8; ++i)
#pragma unroll
        for (int e = 0; e < 4; ++e) { v[2 * e] += bf_lo(part[i][e]); v[2 * e + 1] += bf_hi(part[i][e]); }
      float t[4];
#pragma unroll
      for (int e = 0; e < 4; ++e) {
        const float give = (lane & 1) ? v[e] : v[4 + e], keep = (lane & 1) ? v[4 + e] : v[e];
        t[e] = keep + dpp<0xB1>(give);
      }
      const int dl = lane & 15, d = 8 * ((dl & 7) >> 2) + 4 * (dl >> 3) + (dl & 3);
      *(LAS u32x2*)(L_KVT + w * 544 + d * 32 + (lane >> 4) * 8) = u32x2{pack2(t[0], t[1]), pack2(t[2], t[3])};
      const float z0 = row_sum(asf(zpart[0])), z1 = row_sum(asf(zpart[1])), z2 = row_sum(asf(zpart[2])), z3 = row_sum(asf(zpart[3]));
      if (dl == 0) *(LAS u32x2*)(L_KVT + w * 544 + 512 + (lane >> 4) * 8) = u32x2{pack2(z0, z1), pack2(z2, z3)};
    }
    wg_barrier();
    STAMP(8);
#pragma unroll
    for (int s = 0; s < 2; ++s) {
      const unsigned a = L_KVT + (2 * wn + s) * 544 + (r < 16 ? r : 16) * 32 + 16 * hh;
      f32x16 o = mfma32(lds_r128(a), fq[s][0], zero16);
      o = mfma32(lds_r128(a + 4 * 544), fq[s][1], o);
      store_frag(c, tb, 112 + 4 * ct + 2 * wn + s, pack8(o, 0, __builtin_amdgcn_rcpf(o[8] + eps[s])));
    }
    wait_vm<2>();
  }
  STAMP(9);
}

constexpr int PA = 6, RING = 8, J_FLAG = 7, J_CHECK = 11;
constexpr int frag_step(int f) { return f < PA ? -1 : f < 16 ? f - PA : f < 20 ? J_CHECK + (f - 16) / 2 : f - 7; }
constexpr int n_frag(int j) { int n = 0; for (int f = 0; f < 36; ++f) n += frag_step(f) == j; return n; }
constexpr int n_refill(int j) { return j >= 2 && j <= 19; }
constexpr int n_ops(int j) { return j < 0 ? PA + 1 : (j == J_FLAG) + n_frag(j) + n_refill(j); }
constexpr int ops_between(int a, int b) { int n = 0; for (int t = a; t < b; ++t) n += n_ops(t); return n; }
constexpr int vm_frag(int f) {
  const int s = frag_step(f);
  int n = s < 0 ? 1 : n_refill(s);
  for (int g = f + 1; g < 36; ++g) n += frag_step(g) == s;
  return n + ops_between(s + 1, f);
}
constexpr bool p2_barrier(int j) { return (j % 3 == 2 && j <= 17) || j == 27; }

DI void phase2(const Args& p, Ctx& c, int c16) {
  const int lane = c.lane, w = c.w, q = w & 3, m = w >> 2, r = lane & 31, hh = lane >> 5;
  const int blk = (lane >> 4) & 1, q4 = (lane & 15) >> 2, p4 = lane & 3;
  const unsigned va = lane * 16, sa = WS_AFRAG + (2 * c.rt2 + m) * NKS * 1024;
  const unsigned xo = (c.rt2 * 64 + m * 32 + r) * LD_X + (q * 16 + hh * 8) * 2;
  u32x4 af[RING], xin;
  flag_check(c, c.fe_off, 8);
  static_for<0, PA>([&](auto ic) { load16_sc1<0>(af[ic.value], va, c.sws, sa + p2_ks(c, ic.value) * 1024); });
  load16(xin, xo, c.sx, c.col2);
  unsigned rw[3][2];
#pragma unroll
  for (int i = 0; i < 3; ++i)
#pragma unroll
    for (int t = 0; t < 2; ++t) {
      rw[i][t] = i * 65536 + q * 2048 + (4 * hh + q4) * 128 + ((t ^ (q4 >> 1)) * 64) + 32 * blk + 8 * p4;
      pin(rw[i][t]);
    }
  auto read_w2 = [&](int j, int t) {
    const unsigned a = rw[l_step(j) >> 16][t] + (l_step(j) & 65535);
    return lds_tr2(a, a + 1024);
  };
  wait_vm<PA + 1>();
  wg_barrier();
  flag_store(c, WS_FH + c.rt * 128 + c16 * 8);
  STAMP(10);
  if (w == 1 && blockIdx.x == 0) {
    for (unsigned spin = 0; spin < SPIN_CAP; ++spin) {
      u32x4 a, b;
      load16_sc1<0>(a, lane * 32, c.sws, WS_ARR);
      load16_sc1<16>(b, lane * 32, c.sws, WS_ARR);
      wait_for<0>(a); pin(b);
      const bool ok = a[0] == c.epoch[0] && a[1] == c.epoch[1] && a[2] == c.epoch[0] && a[3] == c.epoch[1] &&
                      b[0] == c.epoch[0] && b[1] == c.epoch[1] && b[2] == c.epoch[0] && b[3] == c.epoch[1];
      if (__all(ok)) break;
    }
    store8_sc1(c.epoch, lane == 0 ? 0 : OOB, c.sws, WS_EPOCH);
  }
  const unsigned ex = m * 24576 + lane * 16;
  auto put = [&](int base, int n, const f32x16 (&a)[2]) {
    const int dst = n >> 1, o = (dst & 1) * 8 + (n & 1) * 4;
    const f32x16& t = a[dst >> 1];
    const unsigned slot = ((q - dst - 1) & 3) * 2048;
    if (q != dst) lds_w128(base + ex + dst * 6144 + slot + (n & 1) * 1024, __builtin_bit_cast(u32x4, f32x4{t[o], t[o + 1], t[o + 2], t[o + 3]}));
  };
  f32x16 accb[2] = {}, accs[2] = {};
  u32x4 wf[2][2];
  wf[0][0] = read_w2(0, 0); wf[0][1] = read_w2(0, 1);
  wf[1][0] = read_w2(1, 0); wf[1][1] = read_w2(1, 1);
  SB();
  static_for<0, 36>([&](auto ic) {
    constexpr int J = decltype(ic)::value;
    f32x16 (&acc)[2] = J < 28 ? accb : accs;
    wait_for<vm_frag(J)>(af[J % RING]);
    if constexpr (J > 28) { pin(acc[0]); pin(acc[1]); }
    SB();
    acc[0] = mfma32(wf[J & 1][0], af[J % RING], acc[0]);
    SB();
    if constexpr (p2_barrier(J + 1)) {
      if constexpr (J + 1 == 17) wait_vm<ops_between(12, J)>();
      if constexpr (J + 1 == 27) wait_vm<ops_between(20, J)>();
      wg_barrier();
      SB();
    }
    if constexpr (J + 2 < 36) wf[J & 1][0] = read_w2(J + 2, 0);
    SB();
    acc[1] = mfma32(wf[J & 1][1], af[J % RING], acc[1]);
    SB();
    if constexpr (J + 2 < 36) wf[J & 1][1] = read_w2(J + 2, 1);
    if constexpr (J == J_FLAG) flag_load(c, c.fh_off, 16);
    if constexpr (J == J_CHECK) {
      wait_vm<n_frag(J_FLAG) + n_refill(J_FLAG) + ops_between(J_FLAG + 1, J)>();
      STAMP(11);
      flag_check(c, c.fh_off, 16);
      STAMP(12);
    }
    static_for<PA, 36>([&](auto fc) {
      if constexpr (frag_step(fc.value) == J) load16_sc1<0>(af[fc.value % RING], va, c.sws, sa + p2_ks(c, fc.value) * 1024);
    });
    if constexpr (n_refill(J)) issue_w2(c, J + 16);
    if constexpr (J >= 28) put(0, J - 28, accb);
    SB();
  });
  pin(xin); pin(accs[0]); pin(accs[1]);
  STAMP(13);
#pragma unroll
  for (int n = 0; n < 8; ++n) put(49152, n, accs);
  float b[8], sv[8];
#pragma unroll
  for (int e = 0; e < 8; ++e) {
    const float b0 = (q & 1) ? accb[0][8 + e] : accb[0][e], b1 = (q & 1) ? accb[1][8 + e] : accb[1][e];
    const float s0 = (q & 1) ? accs[0][8 + e] : accs[0][e], s1 = (q & 1) ? accs[1][8 + e] : accs[1][e];
    b[e] = (q & 2) ? b1 : b0;
    sv[e] = (q & 2) ? s1 : s0;
  }
  wg_barrier();
#pragma unroll
  for (int s = 0; s < 3; ++s) {
    const unsigned a = ex + q * 6144 + s * 2048;
    const f32x4 t0 = __builtin_bit_cast(f32x4, lds_r128(a)), t1 = __builtin_bit_cast(f32x4, lds_r128(a + 1024));
    const f32x4 t2 = __builtin_bit_cast(f32x4, lds_r128(a + 49152)), t3 = __builtin_bit_cast(f32x4, lds_r128(a + 50176));
#pragma unroll
    for (int e = 0; e < 4; ++e) { b[e] += t0[e]; b[4 + e] += t1[e]; sv[e] += t2[e]; sv[4 + e] += t3[e]; }
  }
  float y[8];
#pragma unroll
  for (int e = 0; e < 8; ++e) {
    const float sg = __builtin_amdgcn_rcpf(1.f + __builtin_amdgcn_exp2f(b[e] * -1.44269504088896f));
    y[e] = sv[e] * (b[e] * sg);
  }
#pragma unroll
  for (int e = 0; e < 4; ++e) {
    const auto t = __builtin_amdgcn_permlane32_swap(asu(y[e]), asu(y[4 + e]), false, false);
    y[e] = asf(t[0]);
    y[4 + e] = asf(t[1]);
  }
  u32x4 o;
#pragma unroll
  for (int e = 0; e < 4; ++e) o[e] = pack2(bf_lo(xin[e]) + y[2 * e], bf_hi(xin[e]) + y[2 * e + 1]);
  *(GAS u32x4*)(p.out + xo + c.col2) = o;
  STAMP(14);
}

__global__ __launch_bounds__(512, 2) void hh_fused_kernel(Args p) {
  extern __shared__ __attribute__((aligned(16))) char smem[];
  Ctx c;
  STAMP(0);
  c.lane = threadIdx.x & 63;
  c.w = __builtin_amdgcn_readfirstlane(threadIdx.x >> 6);
  const int xcd = blockIdx.x & 7, j = blockIdx.x >> 3;
  c.rt = 8 * (xcd & 1) + (j & 7);
  c.ct = 2 * (xcd >> 1) + (j >> 4);
  c.rt2 = 4 * xcd + (j & 3);
  c.col2 = (j >> 2) * 128;
  c.xrow = c.rt * 128 * LD_X;
  c.sx = make_srd(p.x, 2048 * LD_X);
  c.sws = make_srd(p.ws, WS_BYTES);
  c.sd = make_srd(p.wd, 1792 * LD_W2);
  c.so = make_srd(p.wo, 512 * LD_W2);
  c.sq = make_srd(p.wqkv, 512 * LD_QKV);
  c.sg = make_srd(p.wg, 512 * LD_GU);
  c.su = make_srd(p.wu, 512 * LD_GU);
  if (CODE_PF) {
    u64 pc;
    asm volatile("s_getpc_b64 %0" : "=s"(pc));
    const int line = j * 16 + (c.lane & 15);
    dma4(L_SINK, (c.w == 7 && c.lane < 16) ? line * 128 : OOB, make_srd((const char*)(pc & ~255ull), CODE_BYTES), 0);
  }
  c.fe_off = WS_FE + (c.rt2 >> 1) * 128;
  c.fh_off = WS_FH + (c.rt2 >> 1) * 128;
  asm volatile("" : "+s"(c.fe_off), "+s"(c.fh_off));
  c.rot1 = ROT1 ? 4 * ((j ^ (j >> 3) ^ (j >> 4)) & 1) : 0;
  c.rot2 = ROT2 ? ((j >> 2) + 2 * (j & 3)) & 7 : 0;
  if ((j >> 3) & 1) phase1<true>(p, c);
  else phase1<false>(p, c);
  phase2(p, c, ((j >> 3) & 1) * 8 + c.ct);
  STAMP_DUMP();
}

extern "C" void kernel_launch(void* const* d_in, const int* in_sizes, int n_in, void* d_out, int out_size,
                              void* d_ws, size_t ws_size, hipStream_t stream) {
  Args a;
  a.x = (const char*)d_in[0]; a.wqkv = (const char*)d_in[1]; a.wphi = (const char*)d_in[2]; a.wo = (const char*)d_in[3];
  a.wg = (const char*)d_in[5]; a.wu = (const char*)d_in[6]; a.wd = (const char*)d_in[7];
  a.ws = (char*)d_ws; a.out = (char*)d_out;
  (void)hipFuncSetAttribute((const void*)hh_fused_kernel, hipFuncAttributeMaxDynamicSharedMemorySize, LDS_BYTES);
  hh_fused_kernel<<<256, 512, LDS_BYTES, stream>>>(a);
}
```

```cpp
#include <hip/hip_runtime.h>
#include <hip/hip_bf16.h>
#include <stdint.h>

typedef unsigned long long u64;
typedef __attribute__((ext_vector_type(8))) __bf16 b16x8;
typedef __attribute__((ext_vector_type(2))) __bf16 b16x2;
typedef __attribute__((ext_vector_type(4))) short s16x4;
typedef __attribute__((ext_vector_type(2))) float f32x2;
typedef __attribute__((ext_vector_type(4))) float f32x4;
typedef __attribute__((ext_vector_type(16))) float f32x16;
typedef __attribute__((ext_vector_type(2))) unsigned u32x2;
typedef __attribute__((ext_vector_type(4))) unsigned u32x4;
typedef __attribute__((ext_vector_type(4))) int i32x4;
#define LAS __attribute__((address_space(3)))
#define GAS __attribute__((address_space(1)))
#define DI __device__ __forceinline__
#define SB() __builtin_amdgcn_sched_barrier(0)

constexpr int LD_X = 1024, LD_QKV = 3072, LD_GU = 3584, LD_W2 = 1024;
constexpr int NKS = 144;
constexpr unsigned WS_AFRAG = 0;
constexpr unsigned WS_KVP = 64u * NKS * 1024;
constexpr unsigned KVP_BYTES = 4608;
constexpr unsigned WS_STAT = WS_KVP + 8 * 16 * KVP_BYTES;
constexpr unsigned WS_FKV = WS_STAT + 2048 * 16;
constexpr unsigned WS_FE = WS_FKV + 8 * 128;
constexpr unsigned WS_FH = WS_FE + 16 * 128;
constexpr unsigned WS_ARR = WS_FH + 16 * 128;
constexpr unsigned WS_EPOCH = WS_ARR + 256 * 8;
constexpr unsigned WS_BYTES = WS_EPOCH + 128;
constexpr unsigned OOB = 0x7ffffff0u;
constexpr unsigned SPIN_CAP = 1u << 20;

constexpr int L_X = 0;
constexpr int L_PK = 0;
constexpr int L_V = 8 * 4352;
constexpr int L_PHI = 53248;
constexpr int L_CS = 55296;
constexpr int L_SINK = 59392;
constexpr int L_KVT = 60416;
constexpr int L_W = 65536;
constexpr int LDS_BYTES = 163840;
template <bool K> constexpr int l_stage(int g) {
  if (K) return g < 8 ? (g % 3) * 16384 : (g - 8) % 3 == 2 ? 24576 : 49152 + ((g - 8) % 3) * 24576;
  return g < 8 ? ((g + 2) % 3) * 32768 : 32768 + (g - 8) * 8192;
}
constexpr int l_step(int j) { return (j % 18 < 6 ? j % 18 : j % 18 + 2) * 8192; }

#define STAMP(n)
#define STAMP_DUMP()

#define CODE_PF 1
#define CODE_BYTES 46000
#define ROT1 0
#define ROT2 1
struct Args {
  const char* x; const char* wqkv; const char* wphi; const char* wo;
  const char* wg; const char* wu; const char* wd;
  char* ws; char* out;
};

DI i32x4 make_srd(const char* p, unsigned bytes) {
  const u64 a = (u64)p;
  i32x4 r;
  r[0] = __builtin_amdgcn_readfirstlane((int)(unsigned)a);
  r[1] = __builtin_amdgcn_readfirstlane((int)(unsigned)(a >> 32) & 0xffff);
  r[2] = (int)bytes;
  r[3] = 0x00020000;
  return r;
}
DI i32x4 sel_srd(bool first, i32x4 a, i32x4 b) {
  i32x4 r;
  const int a0 = a[0], a1 = a[1], a2 = a[2], b0 = b[0], b1 = b[1], b2 = b[2];
  r[0] = first ? a0 : b0; r[1] = first ? a1 : b1; r[2] = first ? a2 : b2; r[3] = 0x00020000;
  return r;
}
DI void dma16(unsigned lds, unsigned voff, i32x4 srd, unsigned soff) {
  asm volatile("s_mov_b32 m0, %0\n\ts_nop 0\n\tbuffer_load_dwordx4 %1, %2, %3 offen lds"
               :: "s"(lds), "v"(voff), "s"(srd), "s"(soff) : "memory");
}
DI void dma4(unsigned lds, unsigned voff, i32x4 srd, unsigned soff) {
  asm volatile("s_mov_b32 m0, %0\n\ts_nop 0\n\tbuffer_load_dword %1, %2, %3 offen lds"
               :: "s"(lds), "v"(voff), "s"(srd), "s"(soff) : "memory");
}
DI void dma16g(unsigned lds, const char* gptr) {
  asm volatile("s_mov_b32 m0, %0\n\ts_nop 0\n\tglobal_load_lds_dwordx4 %1, off" :: "s"(lds), "v"(gptr) : "memory");
}
template <int IMM> DI void load16_sc1(u32x4& d, unsigned voff, i32x4 srd, unsigned soff) {
  asm volatile("buffer_load_dwordx4 %0, %1, %2, %3 offen offset:%4 sc1" : "=v"(d) : "v"(voff), "s"(srd), "s"(soff), "n"(IMM) : "memory");
}
DI void load16(u32x4& d, unsigned voff, i32x4 srd, unsigned soff) {
  asm volatile("buffer_load_dwordx4 %0, %1, %2, %3 offen" : "=v"(d) : "v"(voff), "s"(srd), "s"(soff) : "memory");
}
DI void load8_sc1(u32x2& d, unsigned voff, i32x4 srd, unsigned soff) {
  asm volatile("buffer_load_dwordx2 %0, %1, %2, %3 offen sc1" : "=v"(d) : "v"(voff), "s"(srd), "s"(soff) : "memory");
}
DI void store16_sc1(u32x4 v, unsigned voff, i32x4 srd, unsigned soff) {
  asm volatile("buffer_store_dwordx4 %0, %1, %2, %3 offen sc1\n\ts_nop 1" :: "v"(v), "v"(voff), "s"(srd), "s"(soff) : "memory");
}
DI void store8_sc1(u32x2 v, unsigned voff, i32x4 srd, unsigned soff) {
  asm volatile("buffer_store_dwordx2 %0, %1, %2, %3 offen sc1\n\ts_nop 1" :: "v"(v), "v"(voff), "s"(srd), "s"(soff) : "memory");
}
template <int N> DI void wait_vm() { asm volatile("s_waitcnt vmcnt(%0)" :: "n"(N) : "memory"); }
template <int N, class T> DI void wait_for(T& v) { asm volatile("s_waitcnt vmcnt(%1)" : "+v"(v) : "n"(N) : "memory"); }
template <class T> DI void pin(T& v) { asm volatile("" : "+v"(v)); }
DI void wg_barrier() {
  asm volatile("s_waitcnt lgkmcnt(0)" ::: "memory");
  __builtin_amdgcn_s_barrier();
  asm volatile("" ::: "memory");
}
DI u32x4 lds_r128(unsigned a) { return *(const LAS u32x4*)a; }
DI void lds_w128(unsigned a, u32x4 v) { *(LAS u32x4*)a = v; }
DI u32x4 lds_tr2(unsigned a0, unsigned a1) {
  const s16x4 lo = __builtin_amdgcn_ds_read_tr16_b64_v4i16((LAS s16x4*)a0);
  const s16x4 hi = __builtin_amdgcn_ds_read_tr16_b64_v4i16((LAS s16x4*)a1);
  const u32x2 l = __builtin_bit_cast(u32x2, lo), h = __builtin_bit_cast(u32x2, hi);
  return u32x4{l[0], l[1], h[0], h[1]};
}
DI f32x16 mfma32(u32x4 a, u32x4 b, f32x16 c) {
  return __builtin_amdgcn_mfma_f32_32x32x16_bf16(__builtin_bit_cast(b16x8, a), __builtin_bit_cast(b16x8, b), c, 0, 0, 0);
}
DI f32x4 mfma16(u32x4 a, u32x4 b, f32x4 c) {
  return __builtin_amdgcn_mfma_f32_16x16x32_bf16(__builtin_bit_cast(b16x8, a), __builtin_bit_cast(b16x8, b), c, 0, 0, 0);
}
DI unsigned pack2(float lo, float hi) {
  const b16x2 v = __builtin_convertvector(f32x2{lo, hi}, b16x2);
  return __builtin_bit_cast(unsigned, v);
}
DI float asf(unsigned v) { return __builtin_bit_cast(float, v); }
DI unsigned asu(float v) { return __builtin_bit_cast(unsigned, v); }
DI float bf_lo(unsigned v) { return asf(v << 16); }
DI float bf_hi(unsigned v) { return asf(v & 0xffff0000u); }
DI float half_sum(float v) {
  const auto r = __builtin_amdgcn_permlane32_swap(asu(v), asu(v), false, false);
  return asf(r[0]) + asf(r[1]);
}
DI float half_max(float v) {
  const auto r = __builtin_amdgcn_permlane32_swap(asu(v), asu(v), false, false);
  return fmaxf(asf(r[0]), asf(r[1]));
}
template <int CTRL> DI float dpp(float v) {
  return asf((unsigned)__builtin_amdgcn_update_dpp(0, (int)asu(v), CTRL, 0xf, 0xf, true));
}
DI float row_sum(float s) {
  s += dpp<0xB1>(s); s += dpp<0x4E>(s); s += dpp<0x141>(s); s += dpp<0x140>(s);
  return s;
}
DI float wave_sum(float s) {
  s = row_sum(s);
  const auto r = __builtin_amdgcn_permlane16_swap(asu(s), asu(s), false, false);
  return half_sum(asf(r[0]) + asf(r[1]));
}
DI u32x4 pack8(const f32x16& a, int o, float s) {
  return u32x4{pack2(a[o] * s, a[o + 1] * s), pack2(a[o + 2] * s, a[o + 3] * s),
               pack2(a[o + 4] * s, a[o + 5] * s), pack2(a[o + 6] * s, a[o + 7] * s)};
}
constexpr unsigned ONES2 = 0x3f803f80u;
template <int V> struct IC { static constexpr int value = V; };
template <int I, int N, class F> DI void static_for(F&& f) {
  if constexpr (I < N) { f(IC<I>{}); static_for<I + 1, N>(f); }
}

DI float max3(float a, float b, float c) { float r; asm("v_max3_f32 %0, %1, %2, %3" : "=v"(r) : "v"(a), "v"(b), "v"(c)); return r; }
DI float nmax3(float a, float b, float c) { float r; asm("v_max3_f32 %0, %1, -%2, -%3" : "=v"(r) : "v"(a), "v"(b), "v"(c)); return r; }
DI float max2(float a, float b) { float r; asm("v_max_f32 %0, %1, %2" : "=v"(r) : "v"(a), "v"(b)); return r; }
template <class F> DI float fold2(float a, float b, F op) {
  const auto r = __builtin_amdgcn_permlane32_swap(asu(a), asu(b), false, false);
  return op(asf(r[0]), asf(r[1]));
}
DI void spread2(float v, float& a, float& b) {
  const auto r = __builtin_amdgcn_permlane32_swap(asu(v), asu(v), false, false);
  a = asf(r[0]); b = asf(r[1]);
}
DI void dual_exp(const f32x16& p, int o, f32x2 (&ep)[4], f32x2 (&en)[4], float& sp, float& sn) {
  const float L2E = 1.44269504088896f;
  const float mx = max2(max3(max3(max3(p[o], p[o + 1], p[o + 2]), p[o + 3], p[o + 4]), p[o + 5], p[o + 6]), p[o + 7]);
  const float nm = nmax3(nmax3(nmax3(nmax3(-3.0e38f, p[o], p[o + 1]), p[o + 2], p[o + 3]), p[o + 4], p[o + 5]), p[o + 6], p[o + 7]);
  float a, b;
  spread2(fold2(mx, nm, max2) * -L2E, a, b);
  const f32x2 l2 = {L2E, L2E}, a2 = {a, a}, b2 = {b, b};
#pragma unroll
  for (int i = 0; i < 4; ++i) {
    const f32x2 v = {p[o + 2 * i], p[o + 2 * i + 1]};
    const f32x2 tp = __builtin_elementwise_fma(v, l2, a2), tn = __builtin_elementwise_fma(-v, l2, b2);
    ep[i] = f32x2{__builtin_amdgcn_exp2f(tp[0]), __builtin_amdgcn_exp2f(tp[1])};
    en[i] = f32x2{__builtin_amdgcn_exp2f(tn[0]), __builtin_amdgcn_exp2f(tn[1])};
  }
  const f32x2 s2 = (ep[0] + ep[1]) + (ep[2] + ep[3]), t2 = (en[0] + en[1]) + (en[2] + en[3]);
  sp = s2[0] + s2[1]; sn = t2[0] + t2[1];
}
DI u32x4 pack8s(const f32x2 (&e)[4], float s) {
  u32x4 r;
#pragma unroll
  for (int i = 0; i < 4; ++i) { const f32x2 t = e[i] * s; r[i] = pack2(t[0], t[1]); }
  return r;
}
DI void phi_k(const f32x16& p, int o, u32x4& pos, u32x4& neg) {
  f32x2 ep[4], en[4];
  float sp, sn, rp, rn;
  dual_exp(p, o, ep, en, sp, sn);
  spread2(__builtin_amdgcn_rcpf(fold2(sp, sn, [](float x, float y) { return x + y; })), rp, rn);
  pos = pack8s(ep, rp);
  neg = pack8s(en, rn);
}
DI void phi_q(const f32x16& p, int o, u32x4& pos, u32x4& neg, float& eps) {
  f32x2 ep[4], en[4];
  float sp, sn;
  dual_exp(p, o, ep, en, sp, sn);
  spread2(fold2(sp, sn, [](float x, float y) { return x + y; }), sp, sn);
#pragma unroll
  for (int i = 0; i < 4; ++i) pos[i] = pack2(ep[i][0], ep[i][1]);
  neg = pack8s(en, sp * __builtin_amdgcn_rcpf(sn));
  eps = sp * 1e-6f;
}

enum Op { OP_NONE, OP_X0, OP_X1, OP_W0, OP_W1, OP_W2, OP_W3, OP_TOUCH0, OP_TOUCH1, OP_TOUCH2, OP_STAT, OP_P2W, OP_FE };
struct Slot { int op, arg; };
constexpr int NEVER = 99, N_SLOT = 7;
template <bool K> constexpr Slot slot_at(int t, int n) {
  constexpr int NU = K ? 2 : 4;
  if (t < 8) {
    if (n == 0) return t == 0 ? Slot{OP_TOUCH0, 0} : t == 2 ? Slot{OP_TOUCH1, 0} : t == 4 ? Slot{OP_TOUCH2, 0} : t == 6 ? Slot{OP_STAT, 0} : Slot{OP_NONE, 0};
    if (t < 6) return n < 3 ? Slot{OP_X0 + n - 1, t + 2} : n - 3 < NU ? Slot{OP_W0 + n - 3, t + 2} : Slot{OP_NONE, 0};
    if (K) return n < 4 ? Slot{OP_W0 + n - 1, t + 2} : Slot{OP_NONE, 0};
    return n < 5 ? Slot{OP_W0, 8 + 4 * (t - 6) + n - 1} : Slot{OP_NONE, 0};
  }
  if (!K || t == 8) return Slot{OP_NONE, 0};
  if (t <= 13) return n < 3 ? Slot{OP_W0 + n, t + 2} : n == 3 ? Slot{OP_P2W, t - 9} : (n == 4 && t == 13) ? Slot{OP_P2W, 5} : Slot{OP_NONE, 0};
  if (n < 3) return Slot{OP_P2W, 9 + 3 * (t - 14) + n};
  return (n == 3 && t == 15) ? Slot{OP_FE, 0} : Slot{OP_NONE, 0};
}
template <bool K> constexpr int op_needed_by(Slot s) {
  if (s.op >= OP_X0 && s.op <= OP_W3) return (!K && s.arg > 8) ? 8 : s.arg;
  return s.op == OP_STAT ? 8 : NEVER;
}
template <bool K> constexpr int vm_at(int g) {
  if (g == 0) return K ? 4 : 6;
  if (g == 9) return 8;
  int n = 0;
  for (int t = g - 1; t >= 0; --t) {
    if (t == 8 && g >= 10) return n;
    for (int i = N_SLOT - 1; i >= 0; --i) {
      const Slot s = slot_at<K>(t, i);
      if (s.op != OP_NONE && op_needed_by<K>(s) <= g) return n;
      n += s.op != OP_NONE;
    }
  }
  return n;
}

struct Ctx {
  i32x4 sx, sq, sg, su, sws, sd, so;
  unsigned vx0, vx1, vwq, vwg, vw2;
  const char* vmix;
  unsigned xrow;
  unsigned col2;
  int w, lane, rt, ct, rt2;
  unsigned fe_off, fh_off;
  int rot1, rot2;
  unsigned xa[4];
  unsigned wa[2][4];
  u32x2 epoch, flag;
  u32x4 srow, stat;
};

DI int k_slice(const Ctx& c, int g) { return (g + c.rot1) & 7; }
DI void issue_x(const Ctx& c, int j, int n) {
  dma16(L_X + (j % 3) * 16384 + c.w * 2048 + n * 1024, n ? c.vx1 + 0u : c.vx0 + 0u, c.sx, c.xrow + 128 * k_slice(c, j));
}
template <bool K> DI void issue_w(const Ctx& c, int g, int u) {
  const unsigned l = L_W + l_stage<K>(g) + u * 8192 + c.w * 1024;
  const unsigned k0 = k_slice(c, g) * 64;
  if (K ? g < 8 : g >= 8) dma16(l, c.vwq, c.sq, c.ct * 128 + k0 * LD_QKV + (K ? 1024 + u * 1024 : 0));
  else if (K && u == 2) dma16g(l, c.vmix + k0 * LD_GU);
  else dma16(l, c.vwg, sel_srd(u & 1, c.su, c.sg), k0 * LD_GU + (K ? 2048 + c.ct * 128 : c.ct * 256 + (u >> 1) * 128));
}
DI int p2_ks(const Ctx& c, int j) {
  const int q = c.w & 3;
  if (j < 16) return 16 * q + ((j + 2 * c.rot2) & 15);
  if (j >= 28) return 112 + 8 * q + ((j - 28 + c.rot2) & 7);
  const int t = j - 16 + ((3 * c.rot2) >> 1);
  return 64 + 12 * q + (t >= 12 ? t - 12 : t);
}
DI void issue_w2(const Ctx& c, int j) {
  const int q = c.w & 3, m = c.w >> 2;
  dma16(l_step(j) + q * 2048 + m * 1024, c.vw2, sel_srd(j >= 28, c.so, c.sd),
        c.col2 + ((p2_ks(c, j) - (j >= 28 ? 112 : 0)) * 16 + m * 8) * LD_W2);
}
template <bool K> DI void touch(const Ctx& c, int which) {
  const unsigned sink = L_SINK + (c.w & 3) * 256;
  const int odd = c.w & 1, hw = c.w >> 1, l = c.lane;
  if (which == 2) {
    const bool wo = c.w == 7;
    const int row = wo ? 16 * c.rt2 + l : 56 * c.rt2 + 8 * c.w + l;
    dma4(sink, l < (wo ? 16 : 8) ? row * LD_W2 : OOB, sel_srd(wo, c.so, c.sd), c.col2);
  } else if (K == (which == 0)) {
    const int row = 32 * c.rt + hw * 8 + l;
    dma4(sink, (l < 8 && (K || !odd)) ? row * LD_QKV : OOB, c.sq, c.ct * 128 + (K ? 1024 + odd * 1024 : 0));
  } else {
    const int row = 32 * c.rt + hw * 8 + (l >> 1);
    const unsigned cb = K ? ((l & 1) ? 3072 + 64 * c.ct : 2048 + 128 * c.ct) : 256 * c.ct + (l & 1) * 128;
    dma4(sink, l < 16 ? row * LD_GU + cb : OOB, sel_srd(odd, c.su, c.sg), 0);
  }
}
DI void flag_load(Ctx& c, unsigned off, int n) { load8_sc1(c.flag, (c.lane & (n - 1)) * 8, c.sws, off); }
DI void flag_check(Ctx& c, unsigned off, int n) {
  pin(c.flag);
  for (unsigned spin = 0; spin < SPIN_CAP && !__all(c.flag[0] == c.epoch[0] && c.flag[1] == c.epoch[1]); ++spin) {
    flag_load(c, off, n);
    wait_for<0>(c.flag);
  }
}
DI void flag_store(const Ctx& c, unsigned off) {
  if (c.w == 0) store8_sc1(c.epoch, c.lane == 0 ? 0 : OOB, c.sws, off);
}
template <bool K, int T, int N> DI void run_slot(Ctx& c) {
  constexpr Slot s = slot_at<K>(T, N);
  if constexpr (s.op == OP_X0 || s.op == OP_X1) issue_x(c, s.arg, s.op - OP_X0);
  if constexpr (s.op >= OP_W0 && s.op <= OP_W3) issue_w<K>(c, s.arg, s.op - OP_W0);
  if constexpr (s.op >= OP_TOUCH0 && s.op <= OP_TOUCH2) touch<K>(c, s.op - OP_TOUCH0);
  if constexpr (s.op == OP_STAT) load16_sc1<0>(c.stat, (32 * (c.w & 3) + (c.lane & 31)) * 16, c.sws, WS_STAT + c.rt * 2048);
  if constexpr (s.op == OP_P2W) issue_w2(c, s.arg);
  if constexpr (s.op == OP_FE) flag_load(c, c.fe_off, 8);
}
template <bool K, int G> DI void open_stage(Ctx& c) {
  wait_vm<vm_at<K>(G)>(); wg_barrier();
  if (K && G == 10) flag_store(c, WS_FKV + c.ct * 128 + c.rt * 8);
}
template <bool K> DI u32x4 read_w(const Ctx& c, int g, int i, int u) {
  const int o = l_stage<K>(g) + u * 8192;
  const unsigned a = c.wa[o >= 49152][i] + o % 49152;
  return lds_tr2(a, a + 512);
}

struct RowStat { float v[8]; float s; float mean; };
DI void stat_step(Ctx& c, RowStat& t, int n, int c16) {
  if (n == 0) {
    pin(c.srow); pin(c.epoch);
#pragma unroll
    for (int e = 0; e < 4; ++e) { t.v[2 * e] = bf_lo(c.srow[e]); t.v[2 * e + 1] = bf_hi(c.srow[e]); pin(t.v[2 * e]); pin(t.v[2 * e + 1]); }
  }
  if (n == 1) { t.s = ((t.v[0] + t.v[1]) + (t.v[2] + t.v[3])) + ((t.v[4] + t.v[5]) + (t.v[6] + t.v[7])); pin(t.s); }
  if (n == 2) { t.s = row_sum(t.s); pin(t.s); }
  if (n == 3) {
    const auto r = __builtin_amdgcn_permlane16_swap(asu(t.s), asu(t.s), false, false);
    t.mean = half_sum(asf(r[0]) + asf(r[1])) * (1.f / 512.f); pin(t.mean);
  }
  if (n == 4) {
#pragma unroll
    for (int e = 0; e < 8; ++e) { t.v[e] -= t.mean; pin(t.v[e]); }
  }
  if (n == 5) {
    float a = t.v[0] * t.v[0], b = t.v[1] * t.v[1];
#pragma unroll
    for (int e = 2; e < 8; e += 2) { a = fmaf(t.v[e], t.v[e], a); b = fmaf(t.v[e + 1], t.v[e + 1], b); }
    t.s = a + b; pin(t.s);
  }
  if (n == 6) { t.s = row_sum(t.s); pin(t.s); }
  if (n == 7) {
    const auto r = __builtin_amdgcn_permlane16_swap(asu(t.s), asu(t.s), false, false);
    t.s = __builtin_amdgcn_rsqf(half_sum(asf(r[0]) + asf(r[1])) * (1.f / 512.f) + 1e-5f); pin(t.s);
  }
  if (n == 8) {
    const u64 e = (((u64)c.epoch[1] << 32) | c.epoch[0]) + 1;
    c.epoch = u32x2{(unsigned)e, (unsigned)(e >> 32)};
    store16_sc1(u32x4{asu(t.mean), c.epoch[0], asu(t.s), c.epoch[0]}, c.lane == 0 ? 0 : OOB, c.sws,
                WS_STAT + (c.rt * 128 + c16 * 8 + c.w) * 16);
    if (c.w == 0) store8_sc1(c.epoch, c.lane == 0 ? 0 : OOB, c.sws, WS_ARR + blockIdx.x * 8);
  }
}

template <bool K, int NU, int G0, int PD, class H>
DI void gemm_pass(Ctx& c, u32x4 (&xf)[32], f32x16 (&acc)[NU], float (&cs)[NU], int c16, H&& hook) {
  u32x4 wf[PD][NU];
  RowStat rs;
  if (G0 == 0) xf[0] = lds_r128(c.xa[0]);
#pragma unroll
  for (int u = 0; u < NU; ++u) {
#pragma unroll
    for (int d = 0; d < PD; ++d) wf[d][u] = read_w<K>(c, G0, d, u);
    cs[u] = 0.f;
#pragma unroll
    for (int e = 0; e < 16; ++e) acc[u][e] = 0.f;
  }
  SB();
  static_for<0, 32 * NU>([&](auto ic) {
    constexpr int idx = decltype(ic)::value / NU, u = decltype(ic)::value % NU;
    constexpr int g = G0 + idx / 4, i = idx & 3, gn = G0 + (idx + PD) / 4, in = (idx + PD) & 3;
    constexpr bool staged = K || G0 == 0, opens = i == 3 && g < (K ? 15 : 7);
    u32x4& f = wf[idx % PD][u];
    cs[u] += bf_lo(f[i]);
    cs[u] += bf_hi(f[i]);
    pin(cs[u]);
    SB();
    acc[u] = mfma32(f, xf[idx], acc[u]);
    SB();
    if constexpr (u == 0 && opens) { open_stage<K, g + 1>(c); SB(); }
    if constexpr (u == 0 && G0 == 0 && idx + 1 < 32) xf[idx + 1] = lds_r128(c.xa[(idx + 1) & 3] + (((idx + 1) / 4) % 3) * 16384);
    if constexpr (idx + PD < 32) f = read_w<K>(c, gn, in, u);
    if constexpr (G0 == 0 && u < 2 && 2 * idx + u < 9) stat_step(c, rs, 2 * idx + u, c16);
    if constexpr (staged) {
      constexpr int t = i == 3 ? g + 1 : g, pos = i == 3 ? u : NU * (i + 1) + u;
      constexpr int n = t == 0 ? pos - NU : NU == 2 ? pos - 1 : (pos & 1) ? pos / 2 : -1;
      if constexpr (t < 16 && n >= 0 && n < N_SLOT) run_slot<K, t, n>(c);
    }
    if constexpr (u == NU - 1) hook(IC<idx>{});
    SB();
  });
#pragma unroll
  for (int u = 0; u < NU; ++u) pin(acc[u]);
}

template <int NU> DI void put_colsum(const float (&cs)[NU], int wm, int wn, int r) {
#pragma unroll
  for (int u = 0; u < NU; ++u) {
    const float t = half_sum(cs[u]);
    const float hi = bf_lo(pack2(t, 0.f));
    *(LAS unsigned*)(L_CS + ((u * 2 + wn) * 32 + r) * 16 + wm * 4) = pack2(hi, t - hi);
  }
}
template <int NU> DI void apply_mean(f32x16 (&acc)[NU], u32x4 negm, int wn, int r) {
#pragma unroll
  for (int u = 0; u < NU; ++u) acc[u] = mfma32(lds_r128(L_CS + ((u * 2 + wn) * 32 + r) * 16), negm, acc[u]);
}
DI void store_frag(const Ctx& c, int tb, int ks, u32x4 v) {
  store16_sc1(v, c.lane * 16, c.sws, WS_AFRAG + (tb * NKS + ks) * 1024);
}
DI void store_h(const Ctx& c, int tb, int ks, const f32x16& g, const f32x16& u, float r2) {
  f32x16 h;
#pragma unroll
  for (int e = 0; e < 16; ++e) h[e] = g[e] * u[e];
  store_frag(c, tb, ks, pack8(h, 0, r2));
  store_frag(c, tb, ks + 1, pack8(h, 8, r2));
}

template <bool K>
DI void phase1(const Args& p, Ctx& c) {
  constexpr int NU1 = K ? 2 : 4, NU2 = K ? 3 : 1;
  const int tid = threadIdx.x, lane = c.lane, w = c.w, rt = c.rt, ct = c.ct;
  const int wm = w & 3, wn = w >> 2, r = lane & 31, hh = lane >> 5;
  const int blk = (lane >> 4) & 1, q4 = (lane & 15) >> 2, p4 = lane & 3;
  const int tb = rt * 4 + wm, c16 = (K ? 8 : 0) + ct;
  {
    const int row = 16 * w + (lane >> 3), ch = (lane & 7) ^ ((row >> 1) & 7);
    c.vx0 = row * LD_X + ch * 16;
    c.vx1 = (row + 8) * LD_X + (ch ^ 4) * 16;
    issue_x(c, 0, 0); issue_x(c, 0, 1);
    const int wrow = 8 * w + (lane >> 3), wch = (lane & 7) ^ (((lane >> 4) & 1) << 2);
    c.vw2 = (lane >> 3) * LD_W2 + wch * 16;
    c.vwq = wrow * LD_QKV + wch * 16;
    c.vwg = wrow * LD_GU + wch * 16;
    c.vmix = (((wch >> 1) & 1) ? p.wu : p.wg) + wrow * LD_GU + (1536 + 32 * ct + 16 * (wch >> 2) + 8 * (wch & 1)) * 2;
  }
#pragma unroll
  for (int u = 0; u < NU1; ++u) issue_w<K>(c, 0, u);
  load8_sc1(c.epoch, 0, c.sws, WS_EPOCH);
  load16(c.srow, lane * 16, c.sx, c.xrow + (c16 * 8 + w) * LD_X);
  dma4(L_PHI + w * 256, tid * 4, make_srd(p.wphi, 32 * 512), ct * 2048);
#pragma unroll
  for (int i = 0; i < 4; ++i) {
    const int s = (i + wm) & 3;
    c.xa[i] = L_X + (32 * wm + r) * 128 + (((2 * s + hh) ^ ((r >> 1) & 7)) * 16);
    c.wa[0][i] = L_W + 2048 * s + (8 * hh + q4) * 128 + ((wn ^ (q4 >> 1)) * 64) + 32 * blk + 8 * p4;
    c.wa[1][i] = c.wa[0][i] + 49152;
    pin(c.xa[i]); pin(c.wa[0][i]); pin(c.wa[1][i]);
  }
  __builtin_amdgcn_s_barrier();
  issue_x(c, 1, 0); issue_x(c, 1, 1);
#pragma unroll
  for (int u = 0; u < NU1; ++u) issue_w<K>(c, 1, u);
  open_stage<K, 0>(c);
  STAMP(1);

  u32x4 xf[32];
  f32x16 acc[NU1], a2[NU2];
  float cs[NU1], cs2[NU2];
  gemm_pass<K, NU1, 0, 1>(c, xf, acc, cs, c16, [](auto) {});
  STAMP(2);

  put_colsum<NU1>(cs, wm, wn, r);
  if (!K) wait_vm<8>();
  pin(c.stat);
  for (unsigned spin = 0; spin < SPIN_CAP && !__all(c.stat[1] == c.epoch[0] && c.stat[3] == c.epoch[0]); ++spin) {
    load16_sc1<0>(c.stat, (32 * wm + r) * 16, c.sws, WS_STAT + rt * 2048);
    wait_for<0>(c.stat);
  }
  const float rstd = asf(c.stat[2]), r2 = rstd * rstd;
  u32x4 negm;
  {
    const float nm = -asf(c.stat[0]);
    const float mhi = bf_lo(pack2(nm, 0.f));
    const float mm = hh ? (nm - mhi) : mhi;
    const unsigned m2 = pack2(mm, mm);
    negm = u32x4{m2, m2, m2, m2};
  }
  const f32x16 zero16 = {};
  auto logits = [&](const f32x16& a) {
    const unsigned pa = L_PHI + (2 * wn + blk) * 512 + (4 * hh + q4) * 32 + 8 * p4;
    const u32x4 wp = lds_tr2(pa, pa + 256), z = {0u, 0u, 0u, 0u};
    const f32x16 t = mfma32(blk ? z : wp, pack8(a, 0, rstd), zero16);
    return mfma32(blk ? wp : z, pack8(a, 8, rstd), t);
  };
  wg_barrier();
  apply_mean<NU1>(acc, negm, wn, r);

  if constexpr (K) {
    const unsigned img = hh * 2176 + (32 * wm + r) * 16;
    issue_w<K>(c, 10, 0);
    const f32x16 pl = logits(acc[0]);
    issue_w<K>(c, 10, 1);
#pragma unroll
    for (int s = 0; s < 2; ++s) lds_w128(L_V + (2 * wn + s) * 4352 + img, pack8(acc[1], 8 * s, rstd));
    issue_w<K>(c, 10, 2);
#pragma unroll
    for (int s = 0; s < 2; ++s) {
      u32x4 pos, neg;
      phi_k(pl, 8 * s, pos, neg);
      lds_w128(L_PK + (2 * wn + s) * 4352 + img, pos);
      lds_w128(L_PK + (4 + 2 * wn + s) * 4352 + img, neg);
      issue_w2(c, 6 + s);
    }
    issue_w2(c, 8);
    wg_barrier();
    STAMP(3);
    const unsigned kvp = WS_KVP + (ct * 16 + rt) * KVP_BYTES;
    const unsigned t = (p4 >> 1) * 2176 + (4 * (lane >> 4) + q4) * 16 + (p4 & 1) * 8;
    const u32x4 ones = {ONES2, ONES2, ONES2, ONES2};
    f32x4 kv = {}, z = {};
#pragma unroll
    for (int ks = 0; ks < 4; ++ks) {
      const unsigned ap = L_PK + w * 4352 + t + ks * 512, av = L_V + (w & 3) * 4352 + t + ks * 512;
      const u32x4 fa = lds_tr2(ap, ap + 256), fb = lds_tr2(av, av + 256);
      kv = mfma16(fa, fb, kv);
      z = mfma16(fa, ones, z);
    }
    store8_sc1(u32x2{pack2(kv[0], kv[1]), pack2(kv[2], kv[3])}, tid * 8, c.sws, kvp);
    asm volatile("s_nop 7" : "+v"(z));
    store16_sc1(__builtin_bit_cast(u32x4, z), (lane & 15) == 0 ? (lane >> 4) * 16 : OOB, c.sws, kvp + 4096 + w * 64);
    STAMP(4);
    gemm_pass<K, NU2, 8, 1>(c, xf, a2, cs2, c16, [](auto) {});
    STAMP(6);
    put_colsum<NU2>(cs2, wm, wn, r);
    wg_barrier();
    STAMP(8);
    apply_mean<NU2>(a2, negm, wn, r);
    issue_w2(c, 15); issue_w2(c, 16); issue_w2(c, 17);
    store_h(c, tb, 64 + 4 * ct + 2 * wn, a2[0], a2[1], r2);
    f32x16 h;
#pragma unroll
    for (int e = 0; e < 8; ++e) h[e] = a2[2][e] * a2[2][8 + e];
    store_frag(c, tb, 96 + 2 * ct + wn, pack8(h, 0, r2));
    wait_vm<6>();
  } else {
    issue_w2(c, 0);
    store_h(c, tb, 8 * ct + 2 * wn, acc[0], acc[1], r2);
    issue_w2(c, 1);
    store_h(c, tb, 8 * ct + 4 + 2 * wn, acc[2], acc[3], r2);
    wait_vm<6>();
    wg_barrier();
    STAMP(4);
    gemm_pass<K, NU2, 8, 3>(c, xf, a2, cs2, c16, [&](auto ic) {
      constexpr int idx = decltype(ic)::value;
      if constexpr (idx < 16 && (idx & 1)) issue_w2(c, 2 + idx / 2);
      if constexpr (idx == 20) flag_load(c, WS_FKV + ct * 128, 16);
    });
    STAMP(6);
    put_colsum<NU2>(cs2, wm, wn, r);
    wait_vm<0>();
    flag_check(c, WS_FKV + ct * 128, 16);
    const unsigned kvp = WS_KVP + ct * 16 * KVP_BYTES;
    u32x4 part[8], zpart;
    wg_barrier();
    flag_store(c, WS_FE + rt * 128 + ct * 8);
    apply_mean<NU2>(a2, negm, wn, r);
#pragma unroll
    for (int i = 0; i < 5; ++i) load16_sc1<0>(part[i], (lane & 1) * KVP_BYTES + (tid >> 1) * 16, c.sws, kvp + 2 * i * KVP_BYTES);
    const f32x16 pl = logits(a2[0]);
#pragma unroll
    for (int i = 5; i < 8; ++i) load16_sc1<0>(part[i], (lane & 1) * KVP_BYTES + (tid >> 1) * 16, c.sws, kvp + 2 * i * KVP_BYTES);
    load16_sc1<0>(zpart, (lane & 15) * KVP_BYTES + (lane >> 4) * 16, c.sws, kvp + 4096 + w * 64);
    u32x4 fq[2][2];
    float eps[2];
    phi_q(pl, 0, fq[0][0], fq[0][1], eps[0]);
    pin(fq[0][0]); pin(fq[0][1]);
    issue_w2(c, 10); issue_w2(c, 11); issue_w2(c, 12); issue_w2(c, 13);
    phi_q(pl, 8, fq[1][0], fq[1][1], eps[1]);
    pin(fq[1][0]); pin(fq[1][1]);
    issue_w2(c, 14); issue_w2(c, 15); issue_w2(c, 16); issue_w2(c, 17);
    flag_load(c, c.fe_off, 8);
    wait_vm<9>();
    {
#pragma unroll
      for (int i = 0; i < 8; ++i) pin(part[i]);
      pin(zpart);
      float v[8] = {};
#pragma unroll
      for (int i = 0; i < 8; ++i)
#pragma unroll
        for (int e = 0; e < 4; ++e) { v[2 * e] += bf_lo(part[i][e]); v[2 * e + 1] += bf_hi(part[i][e]); }
      float t[4];
#pragma unroll
      for (int e = 0; e < 4; ++e) {
        const float give = (lane & 1) ? v[e] : v[4 + e], keep = (lane & 1) ? v[4 + e] : v[e];
        t[e] = keep + dpp<0xB1>(give);
      }
      const int dl = lane & 15, d = 8 * ((dl & 7) >> 2) + 4 * (dl >> 3) + (dl & 3);
      *(LAS u32x2*)(L_KVT + w * 544 + d * 32 + (lane >> 4) * 8) = u32x2{pack2(t[0], t[1]), pack2(t[2], t[3])};
      const float z0 = row_sum(asf(zpart[0])), z1 = row_sum(asf(zpart[1])), z2 = row_sum(asf(zpart[2])), z3 = row_sum(asf(zpart[3]));
      if (dl == 0) *(LAS u32x2*)(L_KVT + w * 544 + 512 + (lane >> 4) * 8) = u32x2{pack2(z0, z1), pack2(z2, z3)};
    }
    wg_barrier();
    STAMP(8);
#pragma unroll
    for (int s = 0; s < 2; ++s) {
      const unsigned a = L_KVT + (2 * wn + s) * 544 + (r < 16 ? r : 16) * 32 + 16 * hh;
      f32x16 o = mfma32(lds_r128(a), fq[s][0], zero16);
      o = mfma32(lds_r128(a + 4 * 544), fq[s][1], o);
      store_frag(c, tb, 112 + 4 * ct + 2 * wn + s, pack8(o, 0, __builtin_amdgcn_rcpf(o[8] + eps[s])));
    }
    wait_vm<2>();
  }
  STAMP(9);
}

constexpr int PA = 6, RING = 8, J_FLAG = 7, J_CHECK = 11;
constexpr int frag_step(int f) { return f < PA ? -1 : f < 16 ? f - PA : f < 20 ? J_CHECK + (f - 16) / 2 : f - 7; }
constexpr int n_frag(int j) { int n = 0; for (int f = 0; f < 36; ++f) n += frag_step(f) == j; return n; }
constexpr int n_refill(int j) { return j >= 2 && j <= 19; }
constexpr int n_ops(int j) { return j < 0 ? PA + 1 : (j == J_FLAG) + n_frag(j) + n_refill(j); }
constexpr int ops_between(int a, int b) { int n = 0; for (int t = a; t < b; ++t) n += n_ops(t); return n; }
constexpr int vm_frag(int f) {
  const int s = frag_step(f);
  int n = s < 0 ? 1 : n_refill(s);
  for (int g = f + 1; g < 36; ++g) n += frag_step(g) == s;
  return n + ops_between(s + 1, f);
}
constexpr bool p2_barrier(int j) { return (j % 3 == 2 && j <= 17) || j == 27; }

DI void phase2(const Args& p, Ctx& c, int c16) {
  const int lane = c.lane, w = c.w, q = w & 3, m = w >> 2, r = lane & 31, hh = lane >> 5;
  const int blk = (lane >> 4) & 1, q4 = (lane & 15) >> 2, p4 = lane & 3;
  const unsigned va = lane * 16, sa = WS_AFRAG + (2 * c.rt2 + m) * NKS * 1024;
  const unsigned xo = (c.rt2 * 64 + m * 32 + r) * LD_X + (q * 16 + hh * 8) * 2;
  u32x4 af[RING], xin;
  flag_check(c, c.fe_off, 8);
  static_for<0, PA>([&](auto ic) { load16_sc1<0>(af[ic.value], va, c.sws, sa + p2_ks(c, ic.value) * 1024); });
  load16(xin, xo, c.sx, c.col2);
  unsigned rw[3][2];
#pragma unroll
  for (int i = 0; i < 3; ++i)
#pragma unroll
    for (int t = 0; t < 2; ++t) {
      rw[i][t] = i * 65536 + q * 2048 + (4 * hh + q4) * 128 + ((t ^ (q4 >> 1)) * 64) + 32 * blk + 8 * p4;
      pin(rw[i][t]);
    }
  auto read_w2 = [&](int j, int t) {
    const unsigned a = rw[l_step(j) >> 16][t] + (l_step(j) & 65535);
    return lds_tr2(a, a + 1024);
  };
  wait_vm<PA + 1>();
  wg_barrier();
  flag_store(c, WS_FH + c.rt * 128 + c16 * 8);
  STAMP(10);
  if (w == 1 && blockIdx.x == 0) {
    for (unsigned spin = 0; spin < SPIN_CAP; ++spin) {
      u32x4 a, b;
      load16_sc1<0>(a, lane * 32, c.sws, WS_ARR);
      load16_sc1<16>(b, lane * 32, c.sws, WS_ARR);
      wait_for<0>(a); pin(b);
      const bool ok = a[0] == c.epoch[0] && a[1] == c.epoch[1] && a[2] == c.epoch[0] && a[3] == c.epoch[1] &&
                      b[0] == c.epoch[0] && b[1] == c.epoch[1] && b[2] == c.epoch[0] && b[3] == c.epoch[1];
      if (__all(ok)) break;
    }
    store8_sc1(c.epoch, lane == 0 ? 0 : OOB, c.sws, WS_EPOCH);
  }
  const unsigned ex = m * 24576 + lane * 16;
  auto put = [&](int base, int n, const f32x16 (&a)[2]) {
    const int dst = n >> 1, o = (dst & 1) * 8 + (n & 1) * 4;
    const f32x16& t = a[dst >> 1];
    const unsigned slot = ((q - dst - 1) & 3) * 2048;
    if (q != dst) lds_w128(base + ex + dst * 6144 + slot + (n & 1) * 1024, __builtin_bit_cast(u32x4, f32x4{t[o], t[o + 1], t[o + 2], t[o + 3]}));
  };
  f32x16 accb[2] = {}, accs[2] = {};
  u32x4 wf[2][2];
  wf[0][0] = read_w2(0, 0); wf[0][1] = read_w2(0, 1);
  wf[1][0] = read_w2(1, 0); wf[1][1] = read_w2(1, 1);
  SB();
  static_for<0, 36>([&](auto ic) {
    constexpr int J = decltype(ic)::value;
    f32x16 (&acc)[2] = J < 28 ? accb : accs;
    wait_for<vm_frag(J)>(af[J % RING]);
    if constexpr (J > 28) { pin(acc[0]); pin(acc[1]); }
    SB();
    acc[0] = mfma32(wf[J & 1][0], af[J % RING], acc[0]);
    SB();
    if constexpr (p2_barrier(J + 1)) {
      if constexpr (J + 1 == 17) wait_vm<ops_between(12, J)>();
      if constexpr (J + 1 == 27) wait_vm<ops_between(20, J)>();
      wg_barrier();
      SB();
    }
    if constexpr (J + 2 < 36) wf[J & 1][0] = read_w2(J + 2, 0);
    SB();
    acc[1] = mfma32(wf[J & 1][1], af[J % RING], acc[1]);
    SB();
    if constexpr (J + 2 < 36) wf[J & 1][1] = read_w2(J + 2, 1);
    if constexpr (J == J_FLAG) flag_load(c, c.fh_off, 16);
    if constexpr (J == J_CHECK) {
      wait_vm<n_frag(J_FLAG) + n_refill(J_FLAG) + ops_between(J_FLAG + 1, J)>();
      STAMP(11);
      flag_check(c, c.fh_off, 16);
      STAMP(12);
    }
    static_for<PA, 36>([&](auto fc) {
      if constexpr (frag_step(fc.value) == J) load16_sc1<0>(af[fc.value % RING], va, c.sws, sa + p2_ks(c, fc.value) * 1024);
    });
    if constexpr (n_refill(J)) issue_w2(c, J + 16);
    if constexpr (J >= 28) put(0, J - 28, accb);
    SB();
  });
  pin(xin); pin(accs[0]); pin(accs[1]);
  STAMP(13);
#pragma unroll
  for (int n = 0; n < 8; ++n) put(49152, n, accs);
  float b[8], sv[8];
#pragma unroll
  for (int e = 0; e < 8; ++e) {
    const float b0 = (q & 1) ? accb[0][8 + e] : accb[0][e], b1 = (q & 1) ? accb[1][8 + e] : accb[1][e];
    const float s0 = (q & 1) ? accs[0][8 + e] : accs[0][e], s1 = (q & 1) ? accs[1][8 + e] : accs[1][e];
    b[e] = (q & 2) ? b1 : b0;
    sv[e] = (q & 2) ? s1 : s0;
  }
  wg_barrier();
#pragma unroll
  for (int s = 0; s < 3; ++s) {
    const unsigned a = ex + q * 6144 + s * 2048;
    const f32x4 t0 = __builtin_bit_cast(f32x4, lds_r128(a)), t1 = __builtin_bit_cast(f32x4, lds_r128(a + 1024));
    const f32x4 t2 = __builtin_bit_cast(f32x4, lds_r128(a + 49152)), t3 = __builtin_bit_cast(f32x4, lds_r128(a + 50176));
#pragma unroll
    for (int e = 0; e < 4; ++e) { b[e] += t0[e]; b[4 + e] += t1[e]; sv[e] += t2[e]; sv[4 + e] += t3[e]; }
  }
  float y[8];
#pragma unroll
  for (int e = 0; e < 8; ++e) {
    const float sg = __builtin_amdgcn_rcpf(1.f + __builtin_amdgcn_exp2f(b[e] * -1.44269504088896f));
    y[e] = sv[e] * (b[e] * sg);
  }
#pragma unroll
  for (int e = 0; e < 4; ++e) {
    const auto t = __builtin_amdgcn_permlane32_swap(asu(y[e]), asu(y[4 + e]), false, false);
    y[e] = asf(t[0]);
    y[4 + e] = asf(t[1]);
  }
  u32x4 o;
#pragma unroll
  for (int e = 0; e < 4; ++e) o[e] = pack2(bf_lo(xin[e]) + y[2 * e], bf_hi(xin[e]) + y[2 * e + 1]);
  *(GAS u32x4*)(p.out + xo + c.col2) = o;
  STAMP(14);
}

__global__ __launch_bounds__(512, 2) void hh_fused_kernel(Args p) {
  extern __shared__ __attribute__((aligned(16))) char smem[];
  Ctx c;
  STAMP(0);
  c.lane = threadIdx.x & 63;
  c.w = __builtin_amdgcn_readfirstlane(threadIdx.x >> 6);
  const int xcd = blockIdx.x & 7, j = blockIdx.x >> 3;
  c.rt = 8 * (xcd & 1) + (j & 7);
  c.ct = 2 * (xcd >> 1) + (j >> 4);
  c.rt2 = 4 * xcd + (j & 3);
  c.col2 = (j >> 2) * 128;
  c.xrow = c.rt * 128 * LD_X;
  c.sx = make_srd(p.x, 2048 * LD_X);
  c.sws = make_srd(p.ws, WS_BYTES);
  c.sd = make_srd(p.wd, 1792 * LD_W2);
  c.so = make_srd(p.wo, 512 * LD_W2);
  c.sq = make_srd(p.wqkv, 512 * LD_QKV);
  c.sg = make_srd(p.wg, 512 * LD_GU);
  c.su = make_srd(p.wu, 512 * LD_GU);
  if (CODE_PF) {
    u64 pc;
    asm volatile("s_getpc_b64 %0" : "=s"(pc));
    const int line = j * 16 + (c.lane & 15);
    dma4(L_SINK, (c.w == 7 && c.lane < 16) ? line * 128 : OOB, make_srd((const char*)(pc & ~255ull), CODE_BYTES), 0);
  }
  c.fe_off = WS_FE + (c.rt2 >> 1) * 128;
  c.fh_off = WS_FH + (c.rt2 >> 1) * 128;
  asm volatile("" : "+s"(c.fe_off), "+s"(c.fh_off));
  c.rot1 = ROT1 ? 4 * ((j ^ (j >> 3) ^ (j >> 4)) & 1) : 0;
  c.rot2 = ROT2 ? ((j >> 2) + 2 * (j & 3)) & 7 : 0;
  if ((j >> 3) & 1) phase1<true>(p, c);
  else phase1<false>(p, c);
  phase2(p, c, ((j >> 3) & 1) * 8 + c.ct);
  STAMP_DUMP();
}

__global__ void small_dummy() {}
__global__ __launch_bounds__(512, 2) void big_dummy(int n) {
  extern __shared__ __attribute__((aligned(16))) char smem2[];
  if (n == 12345) asm volatile("v_mov_b32 v250, 0" ::: "v250");
}
extern "C" void kernel_launch(void* const* d_in, const int* in_sizes, int n_in, void* d_out, int out_size,
                              void* d_ws, size_t ws_size, hipStream_t stream) {
  Args a;
  a.x = (const char*)d_in[0]; a.wqkv = (const char*)d_in[1]; a.wphi = (const char*)d_in[2]; a.wo = (const char*)d_in[3];
  a.wg = (const char*)d_in[5]; a.wu = (const char*)d_in[6]; a.wd = (const char*)d_in[7];
  a.ws = (char*)d_ws; a.out = (char*)d_out;
  (void)hipFuncSetAttribute((const void*)hh_fused_kernel, hipFuncAttributeMaxDynamicSharedMemorySize, LDS_BYTES);
  hh_fused_kernel<<<256, 512, LDS_BYTES, stream>>>(a);
}
```

```cpp
#include <hip/hip_runtime.h>
#include <hip/hip_bf16.h>
#include <stdint.h>

typedef unsigned long long u64;
typedef __attribute__((ext_vector_type(8))) __bf16 b16x8;
typedef __attribute__((ext_vector_type(2))) __bf16 b16x2;
typedef __attribute__((ext_vector_type(4))) short s16x4;
typedef __attribute__((ext_vector_type(2))) float f32x2;
typedef __attribute__((ext_vector_type(4))) float f32x4;
typedef __attribute__((ext_vector_type(16))) float f32x16;
typedef __attribute__((ext_vector_type(2))) unsigned u32x2;
typedef __attribute__((ext_vector_type(4))) unsigned u32x4;
typedef __attribute__((ext_vector_type(4))) int i32x4;
#define LAS __attribute__((address_space(3)))
#define GAS __attribute__((address_space(1)))
#define DI __device__ __forceinline__
#define SB() __builtin_amdgcn_sched_barrier(0)

constexpr int LD_X = 1024, LD_QKV = 3072, LD_GU = 3584, LD_W2 = 1024;
constexpr int NKS = 144;
constexpr unsigned WS_AFRAG = 0;
constexpr unsigned WS_KVP = 64u * NKS * 1024;
constexpr unsigned KVP_BYTES = 4608;
constexpr unsigned WS_STAT = WS_KVP + 8 * 16 * KVP_BYTES;
constexpr unsigned WS_FKV = WS_STAT + 2048 * 16;
constexpr unsigned WS_FE = WS_FKV + 8 * 128;
constexpr unsigned WS_FH = WS_FE + 16 * 128;
constexpr unsigned WS_ARR = WS_FH + 16 * 128;
constexpr unsigned WS_EPOCH = WS_ARR + 256 * 8;
constexpr unsigned WS_BYTES = WS_EPOCH + 128;
constexpr unsigned OOB = 0x7ffffff0u;
constexpr unsigned SPIN_CAP = 1u << 20;

constexpr int L_X = 0;
constexpr int L_PK = 0;
constexpr int L_V = 8 * 4352;
constexpr int L_PHI = 53248;
constexpr int L_CS = 55296;
constexpr int L_SINK = 59392;
constexpr int L_KVT = 60416;
constexpr int L_W = 65536;
constexpr int LDS_BYTES = 163840;
template <bool K> constexpr int l_stage(int g) {
  if (K) return g < 8 ? (g % 3) * 16384 : (g - 8) % 3 == 2 ? 24576 : 49152 + ((g - 8) % 3) * 24576;
  return g < 8 ? ((g + 2) % 3) * 32768 : 32768 + (g - 8) * 8192;
}
constexpr int l_step(int j) { return (j % 18 < 6 ? j % 18 : j % 18 + 2) * 8192; }

#define STAMP(n)
#define STAMP_DUMP()

#define ROT1 0
#define ROT2 1
struct Args {
  const char* x; const char* wqkv; const char* wphi; const char* wo;
  const char* wg; const char* wu; const char* wd;
  char* ws; char* out;
};

DI i32x4 make_srd(const char* p, unsigned bytes) {
  const u64 a = (u64)p;
  i32x4 r;
  r[0] = __builtin_amdgcn_readfirstlane((int)(unsigned)a);
  r[1] = __builtin_amdgcn_readfirstlane((int)(unsigned)(a >> 32) & 0xffff);
  r[2] = (int)bytes;
  r[3] = 0x00020000;
  return r;
}
DI i32x4 sel_srd(bool first, i32x4 a, i32x4 b) {
  i32x4 r;
  const int a0 = a[0], a1 = a[1], a2 = a[2], b0 = b[0], b1 = b[1], b2 = b[2];
  r[0] = first ? a0 : b0; r[1] = first ? a1 : b1; r[2] = first ? a2 : b2; r[3] = 0x00020000;
  return r;
}
DI void dma16(unsigned lds, unsigned voff, i32x4 srd, unsigned soff) {
  asm volatile("s_mov_b32 m0, %0\n\ts_nop 0\n\tbuffer_load_dwordx4 %1, %2, %3 offen lds"
               :: "s"(lds), "v"(voff), "s"(srd), "s"(soff) : "memory");
}
DI void dma4(unsigned lds, unsigned voff, i32x4 srd, unsigned soff) {
  asm volatile("s_mov_b32 m0, %0\n\ts_nop 0\n\tbuffer_load_dword %1, %2, %3 offen lds"
               :: "s"(lds), "v"(voff), "s"(srd), "s"(soff) : "memory");
}
DI void dma16g(unsigned lds, const char* gptr) {
  asm volatile("s_mov_b32 m0, %0\n\ts_nop 0\n\tglobal_load_lds_dwordx4 %1, off" :: "s"(lds), "v"(gptr) : "memory");
}
template <int IMM> DI void load16_sc1(u32x4& d, unsigned voff, i32x4 srd, unsigned soff) {
  asm volatile("buffer_load_dwordx4 %0, %1, %2, %3 offen offset:%4 sc1" : "=v"(d) : "v"(voff), "s"(srd), "s"(soff), "n"(IMM) : "memory");
}
DI void load16(u32x4& d, unsigned voff, i32x4 srd, unsigned soff) {
  asm volatile("buffer_load_dwordx4 %0, %1, %2, %3 offen" : "=v"(d) : "v"(voff), "s"(srd), "s"(soff) : "memory");
}
DI void load8_sc1(u32x2& d, unsigned voff, i32x4 srd, unsigned soff) {
  asm volatile("buffer_load_dwordx2 %0, %1, %2, %3 offen sc1" : "=v"(d) : "v"(voff), "s"(srd), "s"(soff) : "memory");
}
DI void store16_sc1(u32x4 v, unsigned voff, i32x4 srd, unsigned soff) {
  asm volatile("buffer_store_dwordx4 %0, %1, %2, %3 offen sc1\n\ts_nop 1" :: "v"(v), "v"(voff), "s"(srd), "s"(soff) : "memory");
}
DI void store8_sc1(u32x2 v, unsigned voff, i32x4 srd, unsigned soff) {
  asm volatile("buffer_store_dwordx2 %0, %1, %2, %3 offen sc1\n\ts_nop 1" :: "v"(v), "v"(voff), "s"(srd), "s"(soff) : "memory");
}
template <int N> DI void wait_vm() { asm volatile("s_waitcnt vmcnt(%0)" :: "n"(N) : "memory"); }
template <int N, class T> DI void wait_for(T& v) { asm volatile("s_waitcnt vmcnt(%1)" : "+v"(v) : "n"(N) : "memory"); }
template <class T> DI void pin(T& v) { asm volatile("" : "+v"(v)); }
DI void wg_barrier() {
  asm volatile("s_waitcnt lgkmcnt(0)" ::: "memory");
  __builtin_amdgcn_s_barrier();
  asm volatile("" ::: "memory");
}
DI u32x4 lds_r128(unsigned a) { return *(const LAS u32x4*)a; }
DI void lds_w128(unsigned a, u32x4 v) { *(LAS u32x4*)a = v; }
DI u32x4 lds_tr2(unsigned a0, unsigned a1) {
  const s16x4 lo = __builtin_amdgcn_ds_read_tr16_b64_v4i16((LAS s16x4*)a0);
  const s16x4 hi = __builtin_amdgcn_ds_read_tr16_b64_v4i16((LAS s16x4*)a1);
  const u32x2 l = __builtin_bit_cast(u32x2, lo), h = __builtin_bit_cast(u32x2, hi);
  return u32x4{l[0], l[1], h[0], h[1]};
}
DI f32x16 mfma32(u32x4 a, u32x4 b, f32x16 c) {
  return __builtin_amdgcn_mfma_f32_32x32x16_bf16(__builtin_bit_cast(b16x8, a), __builtin_bit_cast(b16x8, b), c, 0, 0, 0);
}
DI f32x4 mfma16(u32x4 a, u32x4 b, f32x4 c) {
  return __builtin_amdgcn_mfma_f32_16x16x32_bf16(__builtin_bit_cast(b16x8, a), __builtin_bit_cast(b16x8, b), c, 0, 0, 0);
}
DI unsigned pack2(float lo, float hi) {
  const b16x2 v = __builtin_convertvector(f32x2{lo, hi}, b16x2);
  return __builtin_bit_cast(unsigned, v);
}
DI float asf(unsigned v) { return __builtin_bit_cast(float, v); }
DI unsigned asu(float v) { return __builtin_bit_cast(unsigned, v); }
DI float bf_lo(unsigned v) { return asf(v << 16); }
DI float bf_hi(unsigned v) { return asf(v & 0xffff0000u); }
DI float half_sum(float v) {
  const auto r = __builtin_amdgcn_permlane32_swap(asu(v), asu(v), false, false);
  return asf(r[0]) + asf(r[1]);
}
DI float half_max(float v) {
  const auto r = __builtin_amdgcn_permlane32_swap(asu(v), asu(v), false, false);
  return fmaxf(asf(r[0]), asf(r[1]));
}
template <int CTRL> DI float dpp(float v) {
  return asf((unsigned)__builtin_amdgcn_update_dpp(0, (int)asu(v), CTRL, 0xf, 0xf, true));
}
DI float row_sum(float s) {
  s += dpp<0xB1>(s); s += dpp<0x4E>(s); s += dpp<0x141>(s); s += dpp<0x140>(s);
  return s;
}
DI float wave_sum(float s) {
  s = row_sum(s);
  const auto r = __builtin_amdgcn_permlane16_swap(asu(s), asu(s), false, false);
  return half_sum(asf(r[0]) + asf(r[1]));
}
DI u32x4 pack8(const f32x16& a, int o, float s) {
  return u32x4{pack2(a[o] * s, a[o + 1] * s), pack2(a[o + 2] * s, a[o + 3] * s),
               pack2(a[o + 4] * s, a[o + 5] * s), pack2(a[o + 6] * s, a[o + 7] * s)};
}
constexpr unsigned ONES2 = 0x3f803f80u;
template <int V> struct IC { static constexpr int value = V; };
template <int I, int N, class F> DI void static_for(F&& f) {
  if constexpr (I < N) { f(IC<I>{}); static_for<I + 1, N>(f); }
}

DI float max3(float a, float b, float c) { float r; asm("v_max3_f32 %0, %1, %2, %3" : "=v"(r) : "v"(a), "v"(b), "v"(c)); return r; }
DI float nmax3(float a, float b, float c) { float r; asm("v_max3_f32 %0, %1, -%2, -%3" : "=v"(r) : "v"(a), "v"(b), "v"(c)); return r; }
DI float max2(float a, float b) { float r; asm("v_max_f32 %0, %1, %2" : "=v"(r) : "v"(a), "v"(b)); return r; }
template <class F> DI float fold2(float a, float b, F op) {
  const auto r = __builtin_amdgcn_permlane32_swap(asu(a), asu(b), false, false);
  return op(asf(r[0]), asf(r[1]));
}
DI void spread2(float v, float& a, float& b) {
  const auto r = __builtin_amdgcn_permlane32_swap(asu(v), asu(v), false, false);
  a = asf(r[0]); b = asf(r[1]);
}
DI void dual_exp(const f32x16& p, int o, f32x2 (&ep)[4], f32x2 (&en)[4], float& sp, float& sn) {
  const float L2E = 1.44269504088896f;
  const float mx = max2(max3(max3(max3(p[o], p[o + 1], p[o + 2]), p[o + 3], p[o + 4]), p[o + 5], p[o + 6]), p[o + 7]);
  const float nm = nmax3(nmax3(nmax3(nmax3(-3.0e38f, p[o], p[o + 1]), p[o + 2], p[o + 3]), p[o + 4], p[o + 5]), p[o + 6], p[o + 7]);
  float a, b;
  spread2(fold2(mx, nm, max2) * -L2E, a, b);
  const f32x2 l2 = {L2E, L2E}, a2 = {a, a}, b2 = {b, b};
#pragma unroll
  for (int i = 0; i < 4; ++i) {
    const f32x2 v = {p[o + 2 * i], p[o + 2 * i + 1]};
    const f32x2 tp = __builtin_elementwise_fma(v, l2, a2), tn = __builtin_elementwise_fma(-v, l2, b2);
    ep[i] = f32x2{__builtin_amdgcn_exp2f(tp[0]), __builtin_amdgcn_exp2f(tp[1])};
    en[i] = f32x2{__builtin_amdgcn_exp2f(tn[0]), __builtin_amdgcn_exp2f(tn[1])};
  }
  const f32x2 s2 = (ep[0] + ep[1]) + (ep[2] + ep[3]), t2 = (en[0] + en[1]) + (en[2] + en[3]);
  sp = s2[0] + s2[1]; sn = t2[0] + t2[1];
}
DI u32x4 pack8s(const f32x2 (&e)[4], float s) {
  u32x4 r;
#pragma unroll
  for (int i = 0; i < 4; ++i) { const f32x2 t = e[i] * s; r[i] = pack2(t[0], t[1]); }
  return r;
}
DI void phi_k(const f32x16& p, int o, u32x4& pos, u32x4& neg) {
  f32x2 ep[4], en[4];
  float sp, sn, rp, rn;
  dual_exp(p, o, ep, en, sp, sn);
  spread2(__builtin_amdgcn_rcpf(fold2(sp, sn, [](float x, float y) { return x + y; })), rp, rn);
  pos = pack8s(ep, rp);
  neg = pack8s(en, rn);
}
DI void phi_q(const f32x16& p, int o, u32x4& pos, u32x4& neg, float& eps) {
  f32x2 ep[4], en[4];
  float sp, sn;
  dual_exp(p, o, ep, en, sp, sn);
  spread2(fold2(sp, sn, [](float x, float y) { return x + y; }), sp, sn);
#pragma unroll
  for (int i = 0; i < 4; ++i) pos[i] = pack2(ep[i][0], ep[i][1]);
  neg = pack8s(en, sp * __builtin_amdgcn_rcpf(sn));
  eps = sp * 1e-6f;
}

enum Op { OP_NONE, OP_X0, OP_X1, OP_W0, OP_W1, OP_W2, OP_W3, OP_TOUCH0, OP_TOUCH1, OP_TOUCH2, OP_STAT, OP_P2W, OP_FE };
struct Slot { int op, arg; };
constexpr int NEVER = 99, N_SLOT = 7;
template <bool K> constexpr Slot slot_at(int t, int n) {
  constexpr int NU = K ? 2 : 4;
  if (t < 8) {
    if (n == 0) return t == 0 ? Slot{OP_TOUCH0, 0} : t == 2 ? Slot{OP_TOUCH1, 0} : t == 4 ? Slot{OP_TOUCH2, 0} : t == 6 ? Slot{OP_STAT, 0} : Slot{OP_NONE, 0};
    if (t < 6) return n < 3 ? Slot{OP_X0 + n - 1, t + 2} : n - 3 < NU ? Slot{OP_W0 + n - 3, t + 2} : Slot{OP_NONE, 0};
    if (K) return n < 4 ? Slot{OP_W0 + n - 1, t + 2} : Slot{OP_NONE, 0};
    return n < 5 ? Slot{OP_W0, 8 + 4 * (t - 6) + n - 1} : Slot{OP_NONE, 0};
  }
  if (!K || t == 8) return Slot{OP_NONE, 0};
  if (t <= 13) return n < 3 ? Slot{OP_W0 + n, t + 2} : n == 3 ? Slot{OP_P2W, t - 9} : (n == 4 && t == 13) ? Slot{OP_P2W, 5} : Slot{OP_NONE, 0};
  if (n < 3) return Slot{OP_P2W, 9 + 3 * (t - 14) + n};
  return (n == 3 && t == 15) ? Slot{OP_FE, 0} : Slot{OP_NONE, 0};
}
template <bool K> constexpr int op_needed_by(Slot s) {
  if (s.op >= OP_X0 && s.op <= OP_W3) return (!K && s.arg > 8) ? 8 : s.arg;
  return s.op == OP_STAT ? 8 : NEVER;
}
template <bool K> constexpr int vm_at(int g) {
  if (g == 0) return K ? 4 : 6;
  if (g == 9) return 8;
  int n = 0;
  for (int t = g - 1; t >= 0; --t) {
    if (t == 8 && g >= 10) return n;
    for (int i = N_SLOT - 1; i >= 0; --i) {
      const Slot s = slot_at<K>(t, i);
      if (s.op != OP_NONE && op_needed_by<K>(s) <= g) return n;
      n += s.op != OP_NONE;
    }
  }
  return n;
}

struct Ctx {
  i32x4 sx, sq, sg, su, sws, sd, so;
  unsigned vx0, vx1, vwq, vwg, vw2;
  const char* vmix;
  unsigned xrow;
  unsigned col2;
  int w, lane, rt, ct, rt2;
  unsigned fe_off, fh_off;
  int rot1, rot2;
  unsigned xa[4];
  unsigned wa[2][4];
  u32x2 epoch, flag;
  u32x4 srow, stat;
};

DI int k_slice(const Ctx& c, int g) { return (g + c.rot1) & 7; }
DI void issue_x(const Ctx& c, int j, int n) {
  dma16(L_X + (j % 3) * 16384 + c.w * 2048 + n * 1024, n ? c.vx1 + 0u : c.vx0 + 0u, c.sx, c.xrow + 128 * k_slice(c, j));
}
template <bool K> DI void issue_w(const Ctx& c, int g, int u) {
  const unsigned l = L_W + l_stage<K>(g) + u * 8192 + c.w * 1024;
  const unsigned k0 = k_slice(c, g) * 64;
  if (K ? g < 8 : g >= 8) dma16(l, c.vwq, c.sq, c.ct * 128 + k0 * LD_QKV + (K ? 1024 + u * 1024 : 0));
  else if (K && u == 2) dma16g(l, c.vmix + k0 * LD_GU);
  else dma16(l, c.vwg, sel_srd(u & 1, c.su, c.sg), k0 * LD_GU + (K ? 2048 + c.ct * 128 : c.ct * 256 + (u >> 1) * 128));
}
DI int p2_ks(const Ctx& c, int j) {
  const int q = c.w & 3;
  if (j < 16) return 16 * q + ((j + 2 * c.rot2) & 15);
  if (j >= 28) return 112 + 8 * q + ((j - 28 + c.rot2) & 7);
  const int t = j - 16 + ((3 * c.rot2) >> 1);
  return 64 + 12 * q + (t >= 12 ? t - 12 : t);
}
DI void issue_w2(const Ctx& c, int j) {
  const int q = c.w & 3, m = c.w >> 2;
  dma16(l_step(j) + q * 2048 + m * 1024, c.vw2, sel_srd(j >= 28, c.so, c.sd),
        c.col2 + ((p2_ks(c, j) - (j >= 28 ? 112 : 0)) * 16 + m * 8) * LD_W2);
}
template <bool K> DI void touch(const Ctx& c, int which) {
  const unsigned sink = L_SINK + (c.w & 3) * 256;
  const int odd = c.w & 1, hw = c.w >> 1, l = c.lane;
  if (which == 2) {
    const bool wo = c.w == 7;
    const int row = wo ? 16 * c.rt2 + l : 56 * c.rt2 + 8 * c.w + l;
    dma4(sink, l < (wo ? 16 : 8) ? row * LD_W2 : OOB, sel_srd(wo, c.so, c.sd), c.col2);
  } else if (K == (which == 0)) {
    const int row = 32 * c.rt + hw * 8 + l;
    dma4(sink, (l < 8 && (K || !odd)) ? row * LD_QKV : OOB, c.sq, c.ct * 128 + (K ? 1024 + odd * 1024 : 0));
  } else {
    const int row = 32 * c.rt + hw * 8 + (l >> 1);
    const unsigned cb = K ? ((l & 1) ? 3072 + 64 * c.ct : 2048 + 128 * c.ct) : 256 * c.ct + (l & 1) * 128;
    dma4(sink, l < 16 ? row * LD_GU + cb : OOB, sel_srd(odd, c.su, c.sg), 0);
  }
}
DI void flag_load(Ctx& c, unsigned off, int n) { load8_sc1(c.flag, (c.lane & (n - 1)) * 8, c.sws, off); }
DI void flag_check(Ctx& c, unsigned off, int n) {
  pin(c.flag);
  for (unsigned spin = 0; spin < SPIN_CAP && !__all(c.flag[0] == c.epoch[0] && c.flag[1] == c.epoch[1]); ++spin) {
    flag_load(c, off, n);
    wait_for<0>(c.flag);
  }
}
DI void flag_store(const Ctx& c, unsigned off) {
  if (c.w == 0) store8_sc1(c.epoch, c.lane == 0 ? 0 : OOB, c.sws, off);
}
template <bool K, int T, int N> DI void run_slot(Ctx& c) {
  constexpr Slot s = slot_at<K>(T, N);
  if constexpr (s.op == OP_X0 || s.op == OP_X1) issue_x(c, s.arg, s.op - OP_X0);
  if constexpr (s.op >= OP_W0 && s.op <= OP_W3) issue_w<K>(c, s.arg, s.op - OP_W0);
  if constexpr (s.op >= OP_TOUCH0 && s.op <= OP_TOUCH2) touch<K>(c, s.op - OP_TOUCH0);
  if constexpr (s.op == OP_STAT) load16_sc1<0>(c.stat, (32 * (c.w & 3) + (c.lane & 31)) * 16, c.sws, WS_STAT + c.rt * 2048);
  if constexpr (s.op == OP_P2W) issue_w2(c, s.arg);
  if constexpr (s.op == OP_FE) flag_load(c, c.fe_off, 8);
}
template <bool K, int G> DI void open_stage(Ctx& c) {
  wait_vm<vm_at<K>(G)>(); wg_barrier();
  if (K && G == 10) flag_store(c, WS_FKV + c.ct * 128 + c.rt * 8);
}
template <bool K> DI u32x4 read_w(const Ctx& c, int g, int i, int u) {
  const int o = l_stage<K>(g) + u * 8192;
  const unsigned a = c.wa[o >= 49152][i] + o % 49152;
  return lds_tr2(a, a + 512);
}

struct RowStat { float v[8]; float s; float mean; };
DI void stat_step(Ctx& c, RowStat& t, int n, int c16) {
  if (n == 0) {
    pin(c.srow); pin(c.epoch);
#pragma unroll
    for (int e = 0; e < 4; ++e) { t.v[2 * e] = bf_lo(c.srow[e]); t.v[2 * e + 1] = bf_hi(c.srow[e]); pin(t.v[2 * e]); pin(t.v[2 * e + 1]); }
  }
  if (n == 1) { t.s = ((t.v[0] + t.v[1]) + (t.v[2] + t.v[3])) + ((t.v[4] + t.v[5]) + (t.v[6] + t.v[7])); pin(t.s); }
  if (n == 2) { t.s = row_sum(t.s); pin(t.s); }
  if (n == 3) {
    const auto r = __builtin_amdgcn_permlane16_swap(asu(t.s), asu(t.s), false, false);
    t.mean = half_sum(asf(r[0]) + asf(r[1])) * (1.f / 512.f); pin(t.mean);
  }
  if (n == 4) {
#pragma unroll
    for (int e = 0; e < 8; ++e) { t.v[e] -= t.mean; pin(t.v[e]); }
  }
  if (n == 5) {
    float a = t.v[0] * t.v[0], b = t.v[1] * t.v[1];
#pragma unroll
    for (int e = 2; e < 8; e += 2) { a = fmaf(t.v[e], t.v[e], a); b = fmaf(t.v[e + 1], t.v[e + 1], b); }
    t.s = a + b; pin(t.s);
  }
  if (n == 6) { t.s = row_sum(t.s); pin(t.s); }
  if (n == 7) {
    const auto r = __builtin_amdgcn_permlane16_swap(asu(t.s), asu(t.s), false, false);
    t.s = __builtin_amdgcn_rsqf(half_sum(asf(r[0]) + asf(r[1])) * (1.f / 512.f) + 1e-5f); pin(t.s);
  }
  if (n == 8) {
    const u64 e = (((u64)c.epoch[1] << 32) | c.epoch[0]) + 1;
    c.epoch = u32x2{(unsigned)e, (unsigned)(e >> 32)};
    store16_sc1(u32x4{asu(t.mean), c.epoch[0], asu(t.s), c.epoch[0]}, c.lane == 0 ? 0 : OOB, c.sws,
                WS_STAT + (c.rt * 128 + c16 * 8 + c.w) * 16);
    if (c.w == 0) store8_sc1(c.epoch, c.lane == 0 ? 0 : OOB, c.sws, WS_ARR + blockIdx.x * 8);
  }
}

template <bool K, int NU, int G0, int PD, class H>
DI void gemm_pass(Ctx& c, u32x4 (&xf)[32], f32x16 (&acc)[NU], float (&cs)[NU], int c16, H&& hook) {
  u32x4 wf[PD][NU];
  RowStat rs;
  if (G0 == 0) xf[0] = lds_r128(c.xa[0]);
#pragma unroll
  for (int u = 0; u < NU; ++u) {
#pragma unroll
    for (int d = 0; d < PD; ++d) wf[d][u] = read_w<K>(c, G0, d, u);
    cs[u] = 0.f;
#pragma unroll
    for (int e = 0; e < 16; ++e) acc[u][e] = 0.f;
  }
  SB();
  static_for<0, 32 * NU>([&](auto ic) {
    constexpr int idx = decltype(ic)::value / NU, u = decltype(ic)::value % NU;
    constexpr int g = G0 + idx / 4, i = idx & 3, gn = G0 + (idx + PD) / 4, in = (idx + PD) & 3;
    constexpr bool staged = K || G0 == 0, opens = i == 3 && g < (K ? 15 : 7);
    u32x4& f = wf[idx % PD][u];
    cs[u] += bf_lo(f[i]);
    cs[u] += bf_hi(f[i]);
    pin(cs[u]);
    SB();
    acc[u] = mfma32(f, xf[idx], acc[u]);
    SB();
    if constexpr (u == 0 && opens) { open_stage<K, g + 1>(c); SB(); }
    if constexpr (u == 0 && G0 == 0 && idx + 1 < 32) xf[idx + 1] = lds_r128(c.xa[(idx + 1) & 3] + (((idx + 1) / 4) % 3) * 16384);
    if constexpr (idx + PD < 32) f = read_w<K>(c, gn, in, u);
    if constexpr (G0 == 0 && u < 2 && 2 * idx + u < 9) stat_step(c, rs, 2 * idx + u, c16);
    if constexpr (staged) {
      constexpr int t = i == 3 ? g + 1 : g, pos = i == 3 ? u : NU * (i + 1) + u;
      constexpr int n = t == 0 ? pos - NU : NU == 2 ? pos - 1 : (pos & 1) ? pos / 2 : -1;
      if constexpr (t < 16 && n >= 0 && n < N_SLOT) run_slot<K, t, n>(c);
    }
    if constexpr (u == NU - 1) hook(IC<idx>{});
    SB();
  });
#pragma unroll
  for (int u = 0; u < NU; ++u) pin(acc[u]);
}

template <int NU> DI void put_colsum(const float (&cs)[NU], int wm, int wn, int r) {
#pragma unroll
  for (int u = 0; u < NU; ++u) {
    const float t = half_sum(cs[u]);
    const float hi = bf_lo(pack2(t, 0.f));
    *(LAS unsigned*)(L_CS + ((u * 2 + wn) * 32 + r) * 16 + wm * 4) = pack2(hi, t - hi);
  }
}
template <int NU> DI void apply_mean(f32x16 (&acc)[NU], u32x4 negm, int wn, int r) {
#pragma unroll
  for (int u = 0; u < NU; ++u) acc[u] = mfma32(lds_r128(L_CS + ((u * 2 + wn) * 32 + r) * 16), negm, acc[u]);
}
DI void store_frag(const Ctx& c, int tb, int ks, u32x4 v) {
  store16_sc1(v, c.lane * 16, c.sws, WS_AFRAG + (tb * NKS + ks) * 1024);
}
DI void store_h(const Ctx& c, int tb, int ks, const f32x16& g, const f32x16& u, float r2) {
  f32x16 h;
#pragma unroll
  for (int e = 0; e < 16; ++e) h[e] = g[e] * u[e];
  store_frag(c, tb, ks, pack8(h, 0, r2));
  store_frag(c, tb, ks + 1, pack8(h, 8, r2));
}

template <bool K>
DI void phase1(const Args& p, Ctx& c) {
  constexpr int NU1 = K ? 2 : 4, NU2 = K ? 3 : 1;
  const int tid = threadIdx.x, lane = c.lane, w = c.w, rt = c.rt, ct = c.ct;
  const int wm = w & 3, wn = w >> 2, r = lane & 31, hh = lane >> 5;
  const int blk = (lane >> 4) & 1, q4 = (lane & 15) >> 2, p4 = lane & 3;
  const int tb = rt * 4 + wm, c16 = (K ? 8 : 0) + ct;
  {
    const int row = 16 * w + (lane >> 3), ch = (lane & 7) ^ ((row >> 1) & 7);
    c.vx0 = row * LD_X + ch * 16;
    c.vx1 = (row + 8) * LD_X + (ch ^ 4) * 16;
    issue_x(c, 0, 0); issue_x(c, 0, 1);
    const int wrow = 8 * w + (lane >> 3), wch = (lane & 7) ^ (((lane >> 4) & 1) << 2);
    c.vw2 = (lane >> 3) * LD_W2 + wch * 16;
    c.vwq = wrow * LD_QKV + wch * 16;
    c.vwg = wrow * LD_GU + wch * 16;
    c.vmix = (((wch >> 1) & 1) ? p.wu : p.wg) + wrow * LD_GU + (1536 + 32 * ct + 16 * (wch >> 2) + 8 * (wch & 1)) * 2;
  }
#pragma unroll
  for (int u = 0; u < NU1; ++u) issue_w<K>(c, 0, u);
  load8_sc1(c.epoch, 0, c.sws, WS_EPOCH);
  load16(c.srow, lane * 16, c.sx, c.xrow + (c16 * 8 + w) * LD_X);
  dma4(L_PHI + w * 256, tid * 4, make_srd(p.wphi, 32 * 512), ct * 2048);
#pragma unroll
  for (int i = 0; i < 4; ++i) {
    const int s = (i + wm) & 3;
    c.xa[i] = L_X + (32 * wm + r) * 128 + (((2 * s + hh) ^ ((r >> 1) & 7)) * 16);
    c.wa[0][i] = L_W + 2048 * s + (8 * hh + q4) * 128 + ((wn ^ (q4 >> 1)) * 64) + 32 * blk + 8 * p4;
    c.wa[1][i] = c.wa[0][i] + 49152;
    pin(c.xa[i]); pin(c.wa[0][i]); pin(c.wa[1][i]);
  }
  __builtin_amdgcn_s_barrier();
  issue_x(c, 1, 0); issue_x(c, 1, 1);
#pragma unroll
  for (int u = 0; u < NU1; ++u) issue_w<K>(c, 1, u);
  open_stage<K, 0>(c);
  STAMP(1);

  u32x4 xf[32];
  f32x16 acc[NU1], a2[NU2];
  float cs[NU1], cs2[NU2];
  gemm_pass<K, NU1, 0, 1>(c, xf, acc, cs, c16, [](auto) {});
  STAMP(2);

  put_colsum<NU1>(cs, wm, wn, r);
  if (!K) wait_vm<8>();
  pin(c.stat);
  for (unsigned spin = 0; spin < SPIN_CAP && !__all(c.stat[1] == c.epoch[0] && c.stat[3] == c.epoch[0]); ++spin) {
    load16_sc1<0>(c.stat, (32 * wm + r) * 16, c.sws, WS_STAT + rt * 2048);
    wait_for<0>(c.stat);
  }
  const float rstd = asf(c.stat[2]), r2 = rstd * rstd;
  u32x4 negm;
  {
    const float nm = -asf(c.stat[0]);
    const float mhi = bf_lo(pack2(nm, 0.f));
    const float mm = hh ? (nm - mhi) : mhi;
    const unsigned m2 = pack2(mm, mm);
    negm = u32x4{m2, m2, m2, m2};
  }
  const f32x16 zero16 = {};
  auto logits = [&](const f32x16& a) {
    const unsigned pa = L_PHI + (2 * wn + blk) * 512 + (4 * hh + q4) * 32 + 8 * p4;
    const u32x4 wp = lds_tr2(pa, pa + 256), z = {0u, 0u, 0u, 0u};
    const f32x16 t = mfma32(blk ? z : wp, pack8(a, 0, rstd), zero16);
    return mfma32(blk ? wp : z, pack8(a, 8, rstd), t);
  };
  wg_barrier();
  apply_mean<NU1>(acc, negm, wn, r);

  if constexpr (K) {
    const unsigned img = hh * 2176 + (32 * wm + r) * 16;
    issue_w<K>(c, 10, 0);
    const f32x16 pl = logits(acc[0]);
    issue_w<K>(c, 10, 1);
#pragma unroll
    for (int s = 0; s < 2; ++s) lds_w128(L_V + (2 * wn + s) * 4352 + img, pack8(acc[1], 8 * s, rstd));
    issue_w<K>(c, 10, 2);
#pragma unroll
    for (int s = 0; s < 2; ++s) {
      u32x4 pos, neg;
      phi_k(pl, 8 * s, pos, neg);
      lds_w128(L_PK + (2 * wn + s) * 4352 + img, pos);
      lds_w128(L_PK + (4 + 2 * wn + s) * 4352 + img, neg);
      issue_w2(c, 6 + s);
    }
    issue_w2(c, 8);
    wg_barrier();
    STAMP(3);
    const unsigned kvp = WS_KVP + (ct * 16 + rt) * KVP_BYTES;
    const unsigned t = (p4 >> 1) * 2176 + (4 * (lane >> 4) + q4) * 16 + (p4 & 1) * 8;
    const u32x4 ones = {ONES2, ONES2, ONES2, ONES2};
    f32x4 kv = {}, z = {};
#pragma unroll
    for (int ks = 0; ks < 4; ++ks) {
      const unsigned ap = L_PK + w * 4352 + t + ks * 512, av = L_V + (w & 3) * 4352 + t + ks * 512;
      const u32x4 fa = lds_tr2(ap, ap + 256), fb = lds_tr2(av, av + 256);
      kv = mfma16(fa, fb, kv);
      z = mfma16(fa, ones, z);
    }
    store8_sc1(u32x2{pack2(kv[0], kv[1]), pack2(kv[2], kv[3])}, tid * 8, c.sws, kvp);
    asm volatile("s_nop 7" : "+v"(z));
    store16_sc1(__builtin_bit_cast(u32x4, z), (lane & 15) == 0 ? (lane >> 4) * 16 : OOB, c.sws, kvp + 4096 + w * 64);
    STAMP(4);
    gemm_pass<K, NU2, 8, 1>(c, xf, a2, cs2, c16, [](auto) {});
    STAMP(6);
    put_colsum<NU2>(cs2, wm, wn, r);
    wg_barrier();
    STAMP(8);
    apply_mean<NU2>(a2, negm, wn, r);
    issue_w2(c, 15); issue_w2(c, 16); issue_w2(c, 17);
    store_h(c, tb, 64 + 4 * ct + 2 * wn, a2[0], a2[1], r2);
    f32x16 h;
#pragma unroll
    for (int e = 0; e < 8; ++e) h[e] = a2[2][e] * a2[2][8 + e];
    store_frag(c, tb, 96 + 2 * ct + wn, pack8(h, 0, r2));
    wait_vm<6>();
  } else {
    issue_w2(c, 0);
    store_h(c, tb, 8 * ct + 2 * wn, acc[0], acc[1], r2);
    issue_w2(c, 1);
    store_h(c, tb, 8 * ct + 4 + 2 * wn, acc[2], acc[3], r2);
    wait_vm<6>();
    wg_barrier();
    STAMP(4);
    gemm_pass<K, NU2, 8, 3>(c, xf, a2, cs2, c16, [&](auto ic) {
      constexpr int idx = decltype(ic)::value;
      if constexpr (idx < 16 && (idx & 1)) issue_w2(c, 2 + idx / 2);
      if constexpr (idx == 20) flag_load(c, WS_FKV + ct * 128, 16);
    });
    STAMP(6);
    put_colsum<NU2>(cs2, wm, wn, r);
    wait_vm<0>();
    flag_check(c, WS_FKV + ct * 128, 16);
    const unsigned kvp = WS_KVP + ct * 16 * KVP_BYTES;
    u32x4 part[8], zpart;
    wg_barrier();
    flag_store(c, WS_FE + rt * 128 + ct * 8);
    apply_mean<NU2>(a2, negm, wn, r);
#pragma unroll
    for (int i = 0; i < 5; ++i) load16_sc1<0>(part[i], (lane & 1) * KVP_BYTES + (tid >> 1) * 16, c.sws, kvp + 2 * i * KVP_BYTES);
    const f32x16 pl = logits(a2[0]);
#pragma unroll
    for (int i = 5; i < 8; ++i) load16_sc1<0>(part[i], (lane & 1) * KVP_BYTES + (tid >> 1) * 16, c.sws, kvp + 2 * i * KVP_BYTES);
    load16_sc1<0>(zpart, (lane & 15) * KVP_BYTES + (lane >> 4) * 16, c.sws, kvp + 4096 + w * 64);
    u32x4 fq[2][2];
    float eps[2];
    phi_q(pl, 0, fq[0][0], fq[0][1], eps[0]);
    pin(fq[0][0]); pin(fq[0][1]);
    issue_w2(c, 10); issue_w2(c, 11); issue_w2(c, 12); issue_w2(c, 13);
    phi_q(pl, 8, fq[1][0], fq[1][1], eps[1]);
    pin(fq[1][0]); pin(fq[1][1]);
    issue_w2(c, 14); issue_w2(c, 15); issue_w2(c, 16); issue_w2(c, 17);
    flag_load(c, c.fe_off, 8);
    wait_vm<9>();
    {
#pragma unroll
      for (int i = 0; i < 8; ++i) pin(part[i]);
      pin(zpart);
      float v[8] = {};
#pragma unroll
      for (int i = 0; i < 8; ++i)
#pragma unroll
        for (int e = 0; e < 4; ++e) { v[2 * e] += bf_lo(part[i][e]); v[2 * e + 1] += bf_hi(part[i][e]); }
      float t[4];
#pragma unroll
      for (int e = 0; e < 4; ++e) {
        const float give = (lane & 1) ? v[e] : v[4 + e], keep = (lane & 1) ? v[4 + e] : v[e];
        t[e] = keep + dpp<0xB1>(give);
      }
      const int dl = lane & 15, d = 8 * ((dl & 7) >> 2) + 4 * (dl >> 3) + (dl & 3);
      *(LAS u32x2*)(L_KVT + w * 544 + d * 32 + (lane >> 4) * 8) = u32x2{pack2(t[0], t[1]), pack2(t[2], t[3])};
      const float z0 = row_sum(asf(zpart[0])), z1 = row_sum(asf(zpart[1])), z2 = row_sum(asf(zpart[2])), z3 = row_sum(asf(zpart[3]));
      if (dl == 0) *(LAS u32x2*)(L_KVT + w * 544 + 512 + (lane >> 4) * 8) = u32x2{pack2(z0, z1), pack2(z2, z3)};
    }
    wg_barrier();
    STAMP(8);
#pragma unroll
    for (int s = 0; s < 2; ++s) {
      const unsigned a = L_KVT + (2 * wn + s) * 544 + (r < 16 ? r : 16) * 32 + 16 * hh;
      f32x16 o = mfma32(lds_r128(a), fq[s][0], zero16);
      o = mfma32(lds_r128(a + 4 * 544), fq[s][1], o);
      store_frag(c, tb, 112 + 4 * ct + 2 * wn + s, pack8(o, 0, __builtin_amdgcn_rcpf(o[8] + eps[s])));
    }
    wait_vm<2>();
  }
  STAMP(9);
}

constexpr int PA = 6, RING = 8, J_FLAG = 7, J_CHECK = 11;
constexpr int frag_step(int f) { return f < PA ? -1 : f < 16 ? f - PA : f < 20 ? J_CHECK + (f - 16) / 2 : f - 7; }
constexpr int n_frag(int j) { int n = 0; for (int f = 0; f < 36; ++f) n += frag_step(f) == j; return n; }
constexpr int n_refill(int j) { return j >= 2 && j <= 19; }
constexpr int n_ops(int j) { return j < 0 ? PA + 1 : (j == J_FLAG) + n_frag(j) + n_refill(j); }
constexpr int ops_between(int a, int b) { int n = 0; for (int t = a; t < b; ++t) n += n_ops(t); return n; }
constexpr int vm_frag(int f) {
  const int s = frag_step(f);
  int n = s < 0 ? 1 : n_refill(s);
  for (int g = f + 1; g < 36; ++g) n += frag_step(g) == s;
  return n + ops_between(s + 1, f);
}
constexpr bool p2_barrier(int j) { return (j % 3 == 2 && j <= 17) || j == 27; }

DI void phase2(const Args& p, Ctx& c, int c16) {
  const int lane = c.lane, w = c.w, q = w & 3, m = w >> 2, r = lane & 31, hh = lane >> 5;
  const int blk = (lane >> 4) & 1, q4 = (lane & 15) >> 2, p4 = lane & 3;
  const unsigned va = lane * 16, sa = WS_AFRAG + (2 * c.rt2 + m) * NKS * 1024;
  const unsigned xo = (c.rt2 * 64 + m * 32 + r) * LD_X + (q * 16 + hh * 8) * 2;
  u32x4 af[RING], xin;
  flag_check(c, c.fe_off, 8);
  static_for<0, PA>([&](auto ic) { load16_sc1<0>(af[ic.value], va, c.sws, sa + p2_ks(c, ic.value) * 1024); });
  load16(xin, xo, c.sx, c.col2);
  unsigned rw[3][2];
#pragma unroll
  for (int i = 0; i < 3; ++i)
#pragma unroll
    for (int t = 0; t < 2; ++t) {
      rw[i][t] = i * 65536 + q * 2048 + (4 * hh + q4) * 128 + ((t ^ (q4 >> 1)) * 64) + 32 * blk + 8 * p4;
      pin(rw[i][t]);
    }
  auto read_w2 = [&](int j, int t) {
    const unsigned a = rw[l_step(j) >> 16][t] + (l_step(j) & 65535);
    return lds_tr2(a, a + 1024);
  };
  wait_vm<PA + 1>();
  wg_barrier();
  flag_store(c, WS_FH + c.rt * 128 + c16 * 8);
  STAMP(10);
  if (w == 1 && blockIdx.x == 0) {
    for (unsigned spin = 0; spin < SPIN_CAP; ++spin) {
      u32x4 a, b;
      load16_sc1<0>(a, lane * 32, c.sws, WS_ARR);
      load16_sc1<16>(b, lane * 32, c.sws, WS_ARR);
      wait_for<0>(a); pin(b);
      const bool ok = a[0] == c.epoch[0] && a[1] == c.epoch[1] && a[2] == c.epoch[0] && a[3] == c.epoch[1] &&
                      b[0] == c.epoch[0] && b[1] == c.epoch[1] && b[2] == c.epoch[0] && b[3] == c.epoch[1];
      if (__all(ok)) break;
    }
    store8_sc1(c.epoch, lane == 0 ? 0 : OOB, c.sws, WS_EPOCH);
  }
  const unsigned ex = m * 24576 + lane * 16;
  auto put = [&](int base, int n, const f32x16 (&a)[2]) {
    const int dst = n >> 1, o = (dst & 1) * 8 + (n & 1) * 4;
    const f32x16& t = a[dst >> 1];
    const unsigned slot = ((q - dst - 1) & 3) * 2048;
    if (q != dst) lds_w128(base + ex + dst * 6144 + slot + (n & 1) * 1024, __builtin_bit_cast(u32x4, f32x4{t[o], t[o + 1], t[o + 2], t[o + 3]}));
  };
  f32x16 accb[2] = {}, accs[2] = {};
  u32x4 wf[2][2];
  wf[0][0] = read_w2(0, 0); wf[0][1] = read_w2(0, 1);
  wf[1][0] = read_w2(1, 0); wf[1][1] = read_w2(1, 1);
  SB();
  static_for<0, 36>([&](auto ic) {
    constexpr int J = decltype(ic)::value;
    f32x16 (&acc)[2] = J < 28 ? accb : accs;
    wait_for<vm_frag(J)>(af[J % RING]);
    if constexpr (J > 28) { pin(acc[0]); pin(acc[1]); }
    SB();
    acc[0] = mfma32(wf[J & 1][0], af[J % RING], acc[0]);
    SB();
    if constexpr (p2_barrier(J + 1)) {
      if constexpr (J + 1 == 17) wait_vm<ops_between(12, J)>();
      if constexpr (J + 1 == 27) wait_vm<ops_between(20, J)>();
      wg_barrier();
      SB();
    }
    if constexpr (J + 2 < 36) wf[J & 1][0] = read_w2(J + 2, 0);
    SB();
    acc[1] = mfma32(wf[J & 1][1], af[J % RING], acc[1]);
    SB();
    if constexpr (J + 2 < 36) wf[J & 1][1] = read_w2(J + 2, 1);
    if constexpr (J == J_FLAG) flag_load(c, c.fh_off, 16);
    if constexpr (J == J_CHECK) {
      wait_vm<n_frag(J_FLAG) + n_refill(J_FLAG) + ops_between(J_FLAG + 1, J)>();
      STAMP(11);
      flag_check(c, c.fh_off, 16);
      STAMP(12);
    }
    static_for<PA, 36>([&](auto fc) {
      if constexpr (frag_step(fc.value) == J) load16_sc1<0>(af[fc.value % RING], va, c.sws, sa + p2_ks(c, fc.value) * 1024);
    });
    if constexpr (n_refill(J)) issue_w2(c, J + 16);
    if constexpr (J >= 28) put(0, J - 28, accb);
    SB();
  });
  pin(xin); pin(accs[0]); pin(accs[1]);
  STAMP(13);
#pragma unroll
  for (int n = 0; n < 8; ++n) put(49152, n, accs);
  float b[8], sv[8];
#pragma unroll
  for (int e = 0; e < 8; ++e) {
    const float b0 = (q & 1) ? accb[0][8 + e] : accb[0][e], b1 = (q & 1) ? accb[1][8 + e] : accb[1][e];
    const float s0 = (q & 1) ? accs[0][8 + e] : accs[0][e], s1 = (q & 1) ? accs[1][8 + e] : accs[1][e];
    b[e] = (q & 2) ? b1 : b0;
    sv[e] = (q & 2) ? s1 : s0;
  }
  wg_barrier();
#pragma unroll
  for (int s = 0; s < 3; ++s) {
    const unsigned a = ex + q * 6144 + s * 2048;
    const f32x4 t0 = __builtin_bit_cast(f32x4, lds_r128(a)), t1 = __builtin_bit_cast(f32x4, lds_r128(a + 1024));
    const f32x4 t2 = __builtin_bit_cast(f32x4, lds_r128(a + 49152)), t3 = __builtin_bit_cast(f32x4, lds_r128(a + 50176));
#pragma unroll
    for (int e = 0; e < 4; ++e) { b[e] += t0[e]; b[4 + e] += t1[e]; sv[e] += t2[e]; sv[4 + e] += t3[e]; }
  }
  float y[8];
#pragma unroll
  for (int e = 0; e < 8; ++e) {
    const float sg = __builtin_amdgcn_rcpf(1.f + __builtin_amdgcn_exp2f(b[e] * -1.44269504088896f));
    y[e] = sv[e] * (b[e] * sg);
  }
#pragma unroll
  for (int e = 0; e < 4; ++e) {
    const auto t = __builtin_amdgcn_permlane32_swap(asu(y[e]), asu(y[4 + e]), false, false);
    y[e] = asf(t[0]);
    y[4 + e] = asf(t[1]);
  }
  u32x4 o;
#pragma unroll
  for (int e = 0; e < 4; ++e) o[e] = pack2(bf_lo(xin[e]) + y[2 * e], bf_hi(xin[e]) + y[2 * e + 1]);
  *(GAS u32x4*)(p.out + xo + c.col2) = o;
  STAMP(14);
}

__global__ __launch_bounds__(512, 2) void hh_fused_kernel(Args p) {
  extern __shared__ __attribute__((aligned(16))) char smem[];
  Ctx c;
  STAMP(0);
  c.lane = threadIdx.x & 63;
  c.w = __builtin_amdgcn_readfirstlane(threadIdx.x >> 6);
  const int xcd = blockIdx.x & 7, j = blockIdx.x >> 3;
  c.rt = 8 * (xcd & 1) + (j & 7);
  c.ct = 2 * (xcd >> 1) + (j >> 4);
  c.rt2 = 4 * xcd + (j & 3);
  c.col2 = (j >> 2) * 128;
  c.xrow = c.rt * 128 * LD_X;
  c.sx = make_srd(p.x, 2048 * LD_X);
  c.sws = make_srd(p.ws, WS_BYTES);
  c.sd = make_srd(p.wd, 1792 * LD_W2);
  c.so = make_srd(p.wo, 512 * LD_W2);
  c.sq = make_srd(p.wqkv, 512 * LD_QKV);
  c.sg = make_srd(p.wg, 512 * LD_GU);
  c.su = make_srd(p.wu, 512 * LD_GU);
  c.fe_off = WS_FE + (c.rt2 >> 1) * 128;
  c.fh_off = WS_FH + (c.rt2 >> 1) * 128;
  asm volatile("" : "+s"(c.fe_off), "+s"(c.fh_off));
  c.rot1 = ROT1 ? 4 * ((j ^ (j >> 3) ^ (j >> 4)) & 1) : 0;
  c.rot2 = ROT2 ? ((j >> 2) + 2 * (j & 3)) & 7 : 0;
  if ((j >> 3) & 1) phase1<true>(p, c);
  else phase1<false>(p, c);
  phase2(p, c, ((j >> 3) & 1) * 8 + c.ct);
  STAMP_DUMP();
}

extern "C" void kernel_launch(void* const* d_in, const int* in_sizes, int n_in, void* d_out, int out_size,
                              void* d_ws, size_t ws_size, hipStream_t stream) {
  Args a;
  a.x = (const char*)d_in[0]; a.wqkv = (const char*)d_in[1]; a.wphi = (const char*)d_in[2]; a.wo = (const char*)d_in[3];
  a.wg = (const char*)d_in[5]; a.wu = (const char*)d_in[6]; a.wd = (const char*)d_in[7];
  a.ws = (char*)d_ws; a.out = (char*)d_out;
  (void)hipFuncSetAttribute((const void*)hh_fused_kernel, hipFuncAttributeMaxDynamicSharedMemorySize, LDS_BYTES);
  hh_fused_kernel<<<256, 512, LDS_BYTES, stream>>>(a);
}
```
